```python
import torch

def _softmax(x, axis=-1):
    return torch.softmax(x, dim=axis)
def _gelu(x):
    return torch.nn.functional.gelu(x, approximate="tanh")
def _silu(x):
    return torch.nn.functional.silu(x)
def _sigmoid(x):
    return torch.sigmoid(x)
def _layernorm(x, axis=-1, eps=1e-5):
    m = x.mean(dim=axis, keepdim=True)
    v = x.var(dim=axis, keepdim=True, unbiased=False)
    return (x - m) / torch.sqrt(v + eps)
def _scatter_add(tgt, idx, src, axis=0):
    return tgt.clone().index_add_(axis, idx, src)
def _segment_sum(src, seg, n):
    out = torch.zeros((n,) + src.shape[1:], dtype=src.dtype)
    return out.index_add_(0, seg, src)
def _pad(x, pw):
    flat = tuple(v for pair in reversed(pw) for v in pair)
    return torch.nn.functional.pad(x, flat)

kernel_name = 'interleave_layers__sliding_block__mixer_channel_mlp__prompt'

def setup_inputs():

    _w = (lambda a: (lambda t: t.to(torch.bfloat16) if t.is_floating_point() else t)(torch.as_tensor(a)))
    def _A_setup():
        B, S, D = 1, 2048, 512
        return {
            "x": _w((lambda *s: torch.randn(*s, dtype=torch.bfloat16) * 0.05)(B, S, D)),
            "wqkv": _w((lambda *s: torch.randn(*s, dtype=torch.bfloat16) * 0.05)(D, 3 * D)),
            "wo": _w((lambda *s: torch.randn(*s, dtype=torch.bfloat16) * 0.05)(D, D)),
        }
    _A_in = _A_setup()

    def _B_setup():
        B, S, D, F = 1, 2048, 512, 1792
        return {
            "x": _w((lambda *s: torch.randn(*s, dtype=torch.bfloat16) * 0.05)(B, S, D)),
            "w1": _w((lambda *s: torch.randn(*s, dtype=torch.bfloat16) * 3.2)(D, F) * (D ** -0.5)),
            "b1": _w((lambda *s: torch.randn(*s, dtype=torch.bfloat16) * 0.8)(F)),
            "w2": _w((lambda *s: torch.randn(*s, dtype=torch.bfloat16) * 0.2)(F, D) * (F ** -0.5)),
            "b2": _w((lambda *s: torch.randn(*s, dtype=torch.bfloat16) * 0.2)(D)),
        }
    _B_in = _B_setup()

    return {
        "A_x": _A_in["x"],
        "A_wqkv": _A_in["wqkv"],
        "A_wo": _A_in["wo"],
        "B_x": _B_in["x"],
        "B_w1": _B_in["w1"],
        "B_b1": _B_in["b1"],
        "B_w2": _B_in["w2"],
        "B_b2": _B_in["b2"],
    }

def reference(A_x, A_wqkv, A_wo, B_x, B_w1, B_b1, B_w2, B_b2):
    def _A_ref(x, wqkv, wo):
        B, S, D = x.shape
        H = 32; Dh = D // H; W = 256
        qkv = (lambda x, *d: x.permute(*d))((x @ wqkv).reshape(B, S, 3, H, Dh), 2, 0, 3, 1, 4)
        q, k, v = qkv[0], qkv[1], qkv[2]
        scores = torch.einsum("bhqd,bhkd->bhqk", q, k) * (Dh ** -0.5)
        idx = torch.arange(S)
        mask = (idx[:, None] - idx[None, :] >= 0) & (idx[:, None] - idx[None, :] < W)
        scores = torch.where(mask[None, None], scores, -1e9)
        p = _softmax(scores, axis=-1)
        o = torch.einsum("bhqk,bhkd->bhqd", p, v)
        return (lambda x, *d: x.permute(*d))(o, 0, 2, 1, 3).reshape(B, S, D) @ wo

    def _B_ref(x, w1, b1, w2, b2):
        y = _layernorm(x, axis=-1)
        h = _gelu(torch.einsum("bsd,df->bsf", y, w1) + b1)
        return torch.einsum("bsf,fd->bsd", h, w2) + b2

    x = A_x
    x = x + _A_ref(_layernorm(x, axis=-1), A_wqkv, A_wo)
    x = x + _B_ref(_layernorm(x, axis=-1), B_w1, B_b1, B_w2, B_b2)
    x = x + _A_ref(_layernorm(x, axis=-1), A_wqkv, A_wo)
    x = x + _B_ref(_layernorm(x, axis=-1), B_w1, B_b1, B_w2, B_b2)
    return x
```

```cpp
#include <hip/hip_runtime.h>
#include <hip/hip_bf16.h>
#include <stdint.h>
typedef unsigned short u16;
__device__ __forceinline__ float bf2f(u16 v){ return __uint_as_float(((unsigned)v)<<16); }
__device__ __forceinline__ u16 f2bf(float f){ unsigned u=__float_as_uint(f); u += 0x7fffu + ((u>>16)&1u); return (u16)(u>>16); }
constexpr int S=2048, D=512, F=1792, H=32, DH=16, W=256;

__global__ void ln_k(const u16* x, u16* y, int twice){
  int row=blockIdx.x; int t=threadIdx.x;
  float v[8]; float s=0;
  for(int i=0;i<8;i++){ v[i]=bf2f(x[row*D+t*8+i]); s+=v[i]; }
  for(int pass=0; pass<1+twice; ++pass){
    s=0; for(int i=0;i<8;i++) s+=v[i];
    for(int o=32;o>0;o>>=1) s+=__shfl_xor(s,o);
    float m=s/D; float q=0; for(int i=0;i<8;i++){ float d=v[i]-m; q+=d*d; }
    for(int o=32;o>0;o>>=1) q+=__shfl_xor(q,o);
    float r=1.0f/sqrtf(q/D+1e-5f);
    for(int i=0;i<8;i++) v[i]=(v[i]-m)*r;
  }
  for(int i=0;i<8;i++) y[row*D+t*8+i]=f2bf(v[i]);
}
__global__ void gemm_k(const u16* A,const u16* B,const u16* bias,const u16* res,u16* C,int M,int N,int K,int act){
  __shared__ float As[16][17], Bs[16][17];
  int tx=threadIdx.x, ty=threadIdx.y; int row=blockIdx.y*16+ty, col=blockIdx.x*16+tx;
  float acc=0;
  for(int k0=0;k0<K;k0+=16){
    As[ty][tx]=bf2f(A[row*K+k0+tx]); Bs[ty][tx]=bf2f(B[(k0+ty)*N+col]);
    __syncthreads();
    for(int k=0;k<16;k++) acc+=As[ty][k]*Bs[k][tx];
    __syncthreads();
  }
  if(bias) acc+=bf2f(bias[col]);
  if(act){ float u=0.7978845608028654f*(acc+0.044715f*acc*acc*acc); acc=0.5f*acc*(1.0f+tanhf(u)); }
  if(res) acc=bf2f(f2bf(acc))+bf2f(res[row*N+col]);
  C[row*N+col]=f2bf(acc);
}
__global__ void attn_k(const u16* qkv,u16* o){
  int q=blockIdx.x*64+threadIdx.x; int h=blockIdx.y;
  float qv[DH]; for(int d=0;d<DH;d++) qv[d]=bf2f(qkv[q*3*D+h*DH+d]);
  int lo=q-W+1; if(lo<0) lo=0;
  float m=-1e30f;
  for(int k=lo;k<=q;k++){ float s=0; for(int d=0;d<DH;d++) s+=qv[d]*bf2f(qkv[k*3*D+D+h*DH+d]); s*=0.25f; m=fmaxf(m,s);}
  float l=0; float acc[DH]; for(int d=0;d<DH;d++) acc[d]=0;
  for(int k=lo;k<=q;k++){ float s=0; for(int d=0;d<DH;d++) s+=qv[d]*bf2f(qkv[k*3*D+D+h*DH+d]); s*=0.25f; float p=expf(s-m); l+=p; for(int d=0;d<DH;d++) acc[d]+=p*bf2f(qkv[k*3*D+2*D+h*DH+d]); }
  for(int d=0;d<DH;d++) o[q*D+h*DH+d]=f2bf(acc[d]/l);
}
extern "C" void kernel_launch(void* const* d_in, const int* in_sizes, int n_in, void* d_out, int out_size, void* d_ws, size_t ws_size, hipStream_t stream){
  const u16* x=(const u16*)d_in[0]; const u16* wqkv=(const u16*)d_in[1]; const u16* wo=(const u16*)d_in[2];
  const u16* w1=(const u16*)d_in[4]; const u16* b1=(const u16*)d_in[5]; const u16* w2=(const u16*)d_in[6]; const u16* b2=(const u16*)d_in[7];
  u16* ws=(u16*)d_ws;
  u16* y=ws; u16* qkv=y+S*D; u16* o=qkv+S*3*D; u16* x1=o+S*D; u16* h=x1+S*D; u16* x2=h+S*F;
  const u16* cur=x;
  for(int L=0;L<2;L++){
    ln_k<<<S,64,0,stream>>>(cur,y,0);
    gemm_k<<<dim3(3*D/16,S/16),dim3(16,16),0,stream>>>(y,wqkv,nullptr,nullptr,qkv,S,3*D,D,0);
    attn_k<<<dim3(S/64,H),64,0,stream>>>(qkv,o);
    gemm_k<<<dim3(D/16,S/16),dim3(16,16),0,stream>>>(o,wo,nullptr,cur,x1,S,D,D,0);
    ln_k<<<S,64,0,stream>>>(x1,y,1);
    gemm_k<<<dim3(F/16,S/16),dim3(16,16),0,stream>>>(y,w1,b1,nullptr,h,S,F,D,1);
    u16* dst = L==0 ? x2 : (u16*)d_out;
    gemm_k<<<dim3(D/16,S/16),dim3(16,16),0,stream>>>(h,w2,b2,x1,dst,S,D,F,0);
    cur=x2;
  }
}
```

```cpp
#include <hip/hip_runtime.h>
#include <stdint.h>

typedef unsigned short u16;
typedef unsigned long long u64;
typedef short bf16x8 __attribute__((ext_vector_type(8)));
typedef short s16x4 __attribute__((ext_vector_type(4)));
typedef float f32x2 __attribute__((ext_vector_type(2)));
typedef float f32x4 __attribute__((ext_vector_type(4)));
typedef float f32x16 __attribute__((ext_vector_type(16)));
typedef unsigned u32x4 __attribute__((ext_vector_type(4)));
typedef unsigned u32x2 __attribute__((ext_vector_type(2)));
typedef __bf16 bf16x2_t __attribute__((ext_vector_type(2)));
#define LAS __attribute__((address_space(3)))
#define GAS __attribute__((address_space(1)))
#define DI __device__ __forceinline__
#define INL __attribute__((always_inline))
typedef __amdgpu_buffer_rsrc_t rsrc_t;

constexpr int S_ = 2048, D_ = 512, F_ = 1792;
constexpr float EPS = 1e-5f;
constexpr float QSCALE = 0.25f * 1.4426950408889634f;
constexpr int NP1 = 14;

constexpr unsigned WS_EPOCH = 0, WS_ERR = 128, WS_FLAGS = 4096;
enum { K_KV = 0, K_O = 1, K_X1 = 2, K_H = 3, K_X2 = 4 };
constexpr unsigned flag_row(int L, int kind) { return WS_FLAGS + (unsigned)((L * 5 + kind) * 256 * 128); }
constexpr unsigned FL_WT = flag_row(1, K_X2);
constexpr unsigned WS_WQT = 0x60000;
constexpr unsigned WS_WOT = WS_WQT + 16 * 96 * 1024;
constexpr unsigned WS_W1T = WS_WOT + 8 * 64 * 1024;
constexpr unsigned WS_W2T = WS_W1T + NP1 * 128 * 1024;
constexpr unsigned WS_KG = WS_W2T + 8 * 224 * 1024;
constexpr unsigned WS_VG = WS_KG + 2 * 256 * 8192;
constexpr unsigned WS_OG = WS_VG + 2 * 256 * 8192;
constexpr unsigned WS_X1 = WS_OG + 2 * 128 * 16 * 1024;
constexpr unsigned WS_ST1 = WS_X1 + 2 * 128 * 16 * 1024;
constexpr unsigned WS_HG = WS_ST1 + 2 * 128 * 16 * 128;
constexpr unsigned WS_X2 = WS_HG + 2 * 128 * 56 * 1024;
constexpr unsigned WS_ST2 = WS_X2 + 128 * 16 * 1024;
constexpr unsigned WS_DBG = WS_ST2 + 128 * 16 * 128;
constexpr unsigned WS_END = WS_DBG + 256 * 8 * 128 * 8;

constexpr int KB = 1024;
constexpr int LDS_BYTES = 163840;
constexpr int T_CSQ = 0;
constexpr int T_CS1 = 512;
constexpr int T_B1 = 1024;
constexpr int T_B2 = 1536;
constexpr int T_DUMP = 1792;
constexpr int MAIN = 2048;
constexpr int PLN = 384 * 16 + 64;
constexpr int A_K = MAIN + 96 * KB, A_V = A_K + 4 * PLN, A_Q = MAIN, A_ONE = A_Q + 4 * 2048;
static_assert(A_V + 4 * PLN <= LDS_BYTES && A_ONE + 6400 <= MAIN + 16 * KB, "attention buffers");
constexpr int L_W1 = MAIN + 16 * KB;
constexpr int L_WO = MAIN + 90 * KB;
constexpr int L_PRE = MAIN + 96 * KB;
constexpr int L_RED2 = MAIN + 96 * KB;
static_assert(L_W1 + 128 * KB <= LDS_BYTES && L_WO + 64 * KB <= LDS_BYTES, "images");

template <int V> struct IC { static constexpr int value = V; };
struct Params {
  const u16 *x, *wqkv, *wo, *w1, *b1, *w2, *b2;
  u16* out;
  unsigned char* ws;
};

DI float bf2f(unsigned v) { return __uint_as_float(v << 16); }
DI float bflo(unsigned v) { return __uint_as_float(v << 16); }
DI float bfhi(unsigned v) { return __uint_as_float(v & 0xffff0000u); }
DI unsigned cvtpk(float lo, float hi) { f32x2 v = {lo, hi}; bf16x2_t b = __builtin_convertvector(v, bf16x2_t); return __builtin_bit_cast(unsigned, b); }
DI unsigned cvtpk2(f32x2 v) { bf16x2_t b = __builtin_convertvector(v, bf16x2_t); return __builtin_bit_cast(unsigned, b); }
DI f32x4 mfma16(bf16x8 a, bf16x8 b, f32x4 c) { return __builtin_amdgcn_mfma_f32_16x16x32_bf16(a, b, c, 0, 0, 0); }
DI f32x16 mfma32(bf16x8 a, bf16x8 b, f32x16 c) { return __builtin_amdgcn_mfma_f32_32x32x16_bf16(a, b, c, 0, 0, 0); }
DI bf16x8 ones_frag() { unsigned o = 0x3F803F80u; asm volatile("" : "+v"(o)); return __builtin_bit_cast(bf16x8, u32x4{o, o, o, o}); }
DI bf16x8 asfrag(u32x4 v) { return __builtin_bit_cast(bf16x8, v); }
DI s16x4 trd(const LAS char* p) { return __builtin_bit_cast(s16x4, __builtin_amdgcn_ds_read_tr16_b64_v4i16((LAS s16x4*)p)); }
DI bf16x8 ldsfrag(const LAS char* p) { return *(const LAS bf16x8*)p; }
DI float max3f(float a, float b, float c) { return __builtin_fmaxf(__builtin_fmaxf(a, b), c); }
DI float swap32_add(float x) { auto r = __builtin_amdgcn_permlane32_swap(__float_as_uint(x), __float_as_uint(x), false, false); return __uint_as_float(r[0]) + __uint_as_float(r[1]); }
DI float swap16_add(float x) { auto r = __builtin_amdgcn_permlane16_swap(__float_as_uint(x), __float_as_uint(x), false, false); return __uint_as_float(r[0]) + __uint_as_float(r[1]); }
DI float quad_sum(float x) { return swap32_add(swap16_add(x)); }
DI float swap32_max(float x) { auto r = __builtin_amdgcn_permlane32_swap(__float_as_uint(x), __float_as_uint(x), false, false); return fmaxf(__uint_as_float(r[0]), __uint_as_float(r[1])); }

#define FRESH() do { asm volatile("" : "+v"(lane), "+s"(w), "+s"(t), "+s"(p)); tok = lane & 15; g = lane >> 4; hh = (p >> 2) & 1; cg = 4 * (p >> 3) + (p & 3); tt = w & 3; kh = w >> 2; \
  tauA = 8 * t + w; tauB = 8 * t + 4 * hh + tt; ksB = 2 * cg + kh; u32i = 2 * t + hh; } while (0)
#define BAR() asm volatile("s_waitcnt lgkmcnt(0)\n\ts_barrier" ::: "memory")
#define VMWAIT0() asm volatile("s_waitcnt vmcnt(0)" ::: "memory")
#define SBAR() __builtin_amdgcn_sched_barrier(0)
constexpr int SC1 = 16;
DI u32x4 ld128(rsrc_t r, unsigned voff, unsigned soff) { return __builtin_amdgcn_raw_buffer_load_b128(r, voff, soff, SC1); }
DI u32x2 ld64(rsrc_t r, unsigned voff, unsigned soff) { return __builtin_amdgcn_raw_buffer_load_b64(r, voff, soff, SC1); }
DI void st128(u32x4 v, rsrc_t r, unsigned voff, unsigned soff) { __builtin_amdgcn_raw_buffer_store_b128(v, r, voff + soff, 0, SC1); }
DI void st64(u32x2 v, rsrc_t r, unsigned voff, unsigned soff) { __builtin_amdgcn_raw_buffer_store_b64(v, r, voff + soff, 0, SC1); }
DI unsigned rfl(unsigned x) { return __builtin_amdgcn_readfirstlane(x); }
DI void dma16(rsrc_t r, unsigned voff, unsigned soff, unsigned ldsaddr) {
  asm volatile("s_mov_b32 m0, %0\n\ts_nop 3\n\tbuffer_load_dwordx4 %1, %2, %3 offen lds" :: "s"(ldsaddr), "v"(voff), "s"(r), "s"(soff) : "memory");
}
DI void dma16_sc1(rsrc_t r, unsigned voff, unsigned soff, unsigned ldsaddr) {
  asm volatile("s_mov_b32 m0, %0\n\ts_nop 3\n\tbuffer_load_dwordx4 %1, %2, %3 offen sc1 lds" :: "s"(ldsaddr), "v"(voff), "s"(r), "s"(soff) : "memory");
}
DI void touch8k(rsrc_t r, unsigned voff, unsigned soff, unsigned ldsdump) {
  asm volatile("s_mov_b32 m0, %0\n\ts_nop 3\n\tbuffer_load_dword %1, %2, %3 offen sc1 lds" :: "s"(ldsdump), "v"(voff), "s"(r), "s"(soff) : "memory");
}

constexpr unsigned SPIN_CAP = 1u << 14;
DI void poll_flags(rsrc_t rws, unsigned voff, int n, u64 tag, int lane, bool& dead) {
  if (dead) return;
  for (unsigned spins = 0;;) {
    u32x2 v = {(unsigned)tag, (unsigned)(tag >> 32)};
    if (lane < n) v = ld64(rws, voff, 0);
    const bool ok = v.x == (unsigned)tag && v.y == (unsigned)(tag >> 32);
    if (__all(ok)) break;
    __builtin_amdgcn_s_sleep(1);
    if (++spins > SPIN_CAP) { dead = true; if (lane == 0) __builtin_amdgcn_raw_buffer_store_b32(1u, rws, WS_ERR, 0, SC1); break; }
  }
}
DI void set_flag(rsrc_t rws, unsigned off, u64 tag) { u32x2 v = {(unsigned)tag, (unsigned)(tag >> 32)}; st64(v, rws, off, 0); }

template <int NP> DI unsigned raw_voff(int j, int lane, unsigned rowpitch, unsigned cb0, unsigned cbs) {
  const int beta = 2 * j + (lane >> 5), oct = beta / NP, pair = beta % NP, r = (lane >> 2) & 7, q4 = lane & 3;
  return (unsigned)(8 * oct + r) * rowpitch + cb0 + (unsigned)pair * cbs + (unsigned)((((q4 >> 1) ^ (r >> 2) ^ oct) & 1) * 32 + (q4 & 1) * 16);
}
template <int NP, int ORD> DI bf16x8 raw_frag(const LAS char* kbase, int ct, int lane) {
  const int g = lane >> 4, q = (lane & 15) >> 2, pp = lane & 3, pair = ct >> 1, c = ct & 1;
  int o1, r1, o2, r2;
  if (ORD == 0) { o1 = g >> 1; r1 = 4 * (g & 1) + q; o2 = o1 + 2; r2 = r1; }
  else { o1 = 2 * (g >> 1); r1 = 4 * (g & 1) + q; o2 = o1 + 1; r2 = r1; }
  const s16x4 lo = trd(kbase + (o1 * NP + pair) * 512 + r1 * 64 + ((c ^ (r1 >> 2) ^ o1) & 1) * 32 + pp * 8);
  const s16x4 hi = trd(kbase + (o2 * NP + pair) * 512 + r2 * 64 + ((c ^ (r2 >> 2) ^ o2) & 1) * 32 + pp * 8);
  return __builtin_shufflevector(lo, hi, 0, 1, 2, 3, 4, 5, 6, 7);
}

template <int NT, int S0, int S1, int NX, int PD, class XL, class OFF, class HOOK>
DI void gemm_lds(f32x4 (&acc)[NT], bf16x8 (&X)[NX], const LAS char* wb, XL xload, OFF off, HOOK hook) {
  bf16x8 wf[2][NT];
#pragma unroll
  for (int c = 0; c < NT; ++c) wf[0][c] = ldsfrag(wb + off(S0) + c * 1024);
  SBAR();
#pragma unroll
  for (int s = S0; s < S1; ++s) {
    const int cur = (s - S0) & 1;
    if (PD > 0 && s + PD < NX) X[s + PD] = xload(s + PD);
    hook(s);
    SBAR();
#pragma unroll
    for (int c = 0; c < NT; ++c) {
      if (s + 1 < S1) wf[cur ^ 1][c] = ldsfrag(wb + off(s + 1) + c * 1024);
      acc[c] = mfma16(wf[cur][c], X[s], acc[c]);
    }
#pragma unroll
    for (int c = 0; c < NT; ++c) {
      if (s + 1 < S1) __builtin_amdgcn_sched_group_barrier(0x100, 1, 0);
      __builtin_amdgcn_sched_group_barrier(0x008, 1, 0);
    }
    SBAR();
  }
}

#define STAMP(i) do {} while (0)
#define RT(i) do {} while (0)
#define STAMP_OUT() do {} while (0)

DI void stats_load(rsrc_t rws, unsigned base, int lane, u32x2 (&pv)[4]) {
  const int tok = lane & 15, g = lane >> 4;
#pragma unroll
  for (int i = 0; i < 4; ++i) pv[i] = ld64(rws, (unsigned)((4 * g + i) * 128 + tok * 8), base);
}
DI void stats_from_partials(const u32x2 (&pv)[4], float& mean, float& var) {
  float s = 0.f;
#pragma unroll
  for (int i = 0; i < 4; ++i) s += __uint_as_float(pv[i].x);
  s = quad_sum(s);
  mean = s * (1.0f / 512.0f);
  float m2 = 0.f;
#pragma unroll
  for (int i = 0; i < 4; ++i) { const float d = __uint_as_float(pv[i].x) * (1.0f / 32.0f) - mean; m2 += __uint_as_float(pv[i].y) + 32.0f * d * d; }
  m2 = quad_sum(m2);
  var = m2 * (1.0f / 512.0f);
}

DI void publish_x(rsrc_t rws, const float (&v)[8], unsigned xoff, unsigned stoff, int lane, u32x4& packed) {
  packed.x = cvtpk(v[0], v[1]); packed.y = cvtpk(v[2], v[3]); packed.z = cvtpk(v[4], v[5]); packed.w = cvtpk(v[6], v[7]);
  st128(packed, rws, (unsigned)lane * 16u, xoff);
  float r[8] = {bflo(packed.x), bfhi(packed.x), bflo(packed.y), bfhi(packed.y), bflo(packed.z), bfhi(packed.z), bflo(packed.w), bfhi(packed.w)};
  float s = 0.f;
#pragma unroll
  for (int i = 0; i < 8; ++i) s += r[i];
  s = quad_sum(s);
  const float mu = s * (1.0f / 32.0f);
  float m2 = 0.f;
#pragma unroll
  for (int i = 0; i < 8; ++i) { const float d = r[i] - mu; m2 += d * d; }
  m2 = quad_sum(m2);
  if (lane < 16) { u32x2 o = {__float_as_uint(s), __float_as_uint(m2)}; st64(o, rws, (unsigned)lane * 8u, stoff); }
}

template <bool FULL, class HOOK>
DI u32x4 attn_wave(const LAS char* lds, int hd, int qi, int jmin, int lane, HOOK hook) {
  const int ql = lane & 31, h = lane >> 5;
  const bf16x8 Q = ldsfrag(lds + A_Q + (hd * 2 + h) * 2048 + (32 * qi + ql) * 16);
  const LAS char* kb = lds + A_K + (hd * 2 + h) * PLN + (32 * qi + ql) * 16;
  const int G = lane >> 4, q4 = (lane & 15) >> 2, pp = lane & 3;
  const LAS char* vb = lds + ((G & 1) ? A_ONE : A_V + (hd * 2 + (pp >> 1)) * PLN) + (32 * qi + 4 * h + q4) * 16 + (pp & 1) * 8;
  constexpr float NEG = -1e30f;
  const f32x16 zero16 = {0.f,0.f,0.f,0.f,0.f,0.f,0.f,0.f,0.f,0.f,0.f,0.f,0.f,0.f,0.f,0.f};
  bf16x8 Kf[9];
#pragma unroll
  for (int j = 0; j < 9; ++j) if (FULL || j >= jmin) Kf[j] = ldsfrag(kb + j * 512);
  f32x16 biasD;
  const int dq = ql - 4 * h;
#pragma unroll
  for (int r = 0; r < 16; ++r) biasD[r] = ((r & 3) + 8 * (r >> 2) <= dq) ? 0.f : NEG;
  float m0 = NEG, m1 = NEG;
  {
    f32x16 sa, sb;
    sa = mfma32(Kf[8], Q, biasD);
#pragma unroll
    for (int j = 7; j >= 0; --j) {
      f32x16& cur = (j & 1) ? sa : sb; f32x16& nxt = (j & 1) ? sb : sa;
      if (FULL || j >= jmin) {
        if (j == 0) { f32x16 bf;
#pragma unroll
          for (int r = 0; r < 16; ++r) bf[r] = NEG - biasD[r];
          nxt = mfma32(Kf[0], Q, bf); }
        else nxt = mfma32(Kf[j], Q, zero16);
      }
      if (FULL || j + 1 >= jmin) {
#pragma unroll
        for (int r = 0; r < 16; r += 4) { m0 = max3f(m0, cur[r], cur[r + 1]); m1 = max3f(m1, cur[r + 2], cur[r + 3]); }
      }
    }
    if (FULL || 0 >= jmin) {
#pragma unroll
      for (int r = 0; r < 16; r += 4) { m0 = max3f(m0, sb[r], sb[r + 1]); m1 = max3f(m1, sb[r + 2], sb[r + 3]); }
    }
  }
  const float m = swap32_max(fmaxf(m0, m1));
  f32x16 negm;
#pragma unroll
  for (int r = 0; r < 16; ++r) negm[r] = -m;
  f32x16 o = zero16;
  {
    f32x16 sa, sb;
    sa = mfma32(Kf[8], Q, biasD + negm);
#pragma unroll
    for (int j = 8; j >= 0; --j) {
      f32x16& cur = (j & 1) ? sb : sa; f32x16& nxt = (j & 1) ? sa : sb;
      const bool have = FULL || j >= jmin;
      hook(j);
      s16x4 v0, v1, v2, v3;
      if (have) { v0 = trd(vb + (32 * j) * 16); v1 = trd(vb + (32 * j + 8) * 16); v2 = trd(vb + (32 * j + 16) * 16); v3 = trd(vb + (32 * j + 24) * 16); }
      if (j >= 1 && (FULL || j - 1 >= jmin)) {
        if (j - 1 == 0) { f32x16 cf;
#pragma unroll
          for (int r = 0; r < 16; ++r) cf[r] = (NEG - biasD[r]) - m;
          nxt = mfma32(Kf[0], Q, cf); }
        else nxt = mfma32(Kf[j - 1], Q, negm);
      }
      if (have) {
        unsigned pk[8];
#pragma unroll
        for (int r = 0; r < 8; ++r) pk[r] = cvtpk(__builtin_amdgcn_exp2f(cur[2 * r]), __builtin_amdgcn_exp2f(cur[2 * r + 1]));
        o = mfma32(__builtin_shufflevector(v0, v1, 0, 1, 2, 3, 4, 5, 6, 7), asfrag(u32x4{pk[0], pk[1], pk[2], pk[3]}), o);
        o = mfma32(__builtin_shufflevector(v2, v3, 0, 1, 2, 3, 4, 5, 6, 7), asfrag(u32x4{pk[4], pk[5], pk[6], pk[7]}), o);
      }
      SBAR();
    }
  }
  const float inv = __builtin_amdgcn_rcpf(o[8]);
  return u32x4{cvtpk(o[0] * inv, o[1] * inv), cvtpk(o[2] * inv, o[3] * inv), cvtpk(o[4] * inv, o[5] * inv), cvtpk(o[6] * inv, o[7] * inv)};
}

__global__ __launch_bounds__(512, 2) void mega(Params P) {
  __shared__ __attribute__((aligned(16))) char lds_[LDS_BYTES];
  const LAS char* lds = (const LAS char*)lds_;
  LAS char* ldsw = (LAS char*)lds_;
  const unsigned ldsb = (unsigned)(size_t)lds_;
  int w = rfl(threadIdx.x >> 6);
  int lane = threadIdx.x & 63;
  const int b = blockIdx.x;
  int t = 4 * ((b & 7) >> 1) + (b >> 6), p = 8 * (b & 1) + ((b >> 3) & 7);
  int hh = (p >> 2) & 1, cg = 4 * (p >> 3) + (p & 3);
  int tt = w & 3, kh = w >> 2;
  int tauA = 8 * t + w, tauB = 8 * t + 4 * hh + tt, ksB = 2 * cg + kh, u32i = 2 * t + hh;
  int tok = lane & 15, g = lane >> 4;
  const rsrc_t rws = __builtin_amdgcn_make_buffer_rsrc(P.ws, 0, (int)WS_END, 0x00020000);
  bool dead = false;
  STAMP(0); RT(59);
  u64 tag = 0;
  u32x4 resid;
  auto nohook = [](int) INL {};
  auto noload = [](int) INL { return bf16x8{}; };

  auto layer = [&](auto Lc) INL {
    constexpr int L = decltype(Lc)::value;
    FRESH();
    {
      bf16x8 X[16];
      float mean, var;
      const int rotA = 2 * (p & 7);
      f32x4 akv[4];
#pragma unroll
      for (int c = 0; c < 4; ++c) akv[c] = f32x4{0.f, 0.f, 0.f, 0.f};
      if constexpr (L == 0) {
        const rsrc_t rx = __builtin_amdgcn_make_buffer_rsrc((void*)P.x, 0, S_ * D_ * 2, 0x00020000);
        if (w >= 6) touch8k(rx, (unsigned)lane * 128u, rfl((unsigned)(128 * t + 16 * (p & 7)) * 1024u + (unsigned)(w - 6) * 8192u), ldsb + T_DUMP);
        u32x2 epoch_raw = ld64(rws, WS_EPOCH, 0);
        if (w < 2) {
          const rsrc_t rq = __builtin_amdgcn_make_buffer_rsrc((void*)P.wqkv, 0, D_ * 3 * D_ * 2, 0x00020000);
#pragma unroll
          for (int jj = 0; jj < 3; ++jj) { const int j = 3 * w + jj; dma16(rq, raw_voff<3>(j, lane, 3072u, (unsigned)p * 64u, 1024u), rfl((unsigned)t * 32u * 3072u), rfl(ldsb + L_PRE + 18 * KB + (unsigned)j * 1024u)); }
        } else if (w < 4) {
          if (p < NP1) {
            const rsrc_t r1 = __builtin_amdgcn_make_buffer_rsrc((void*)P.w1, 0, D_ * F_ * 2, 0x00020000);
#pragma unroll
            for (int jj = 0; jj < 4; ++jj) { const int j = 4 * (w - 2) + jj; dma16(r1, raw_voff<4>(j, lane, 3584u, (unsigned)p * 256u, 64u), rfl((unsigned)t * 32u * 3584u), rfl(ldsb + L_PRE + (unsigned)j * 1024u)); }
          }
        } else if (w == 4) {
          const rsrc_t ro = __builtin_amdgcn_make_buffer_rsrc((void*)P.wo, 0, D_ * D_ * 2, 0x00020000);
#pragma unroll
          for (int j = 0; j < 2; ++j) dma16(ro, raw_voff<1>(j, lane, 1024u, (unsigned)(cg * 128 + (u32i & 1) * 64), 0u), rfl((unsigned)(u32i >> 1) * 32u * 1024u), rfl(ldsb + L_PRE + 8 * KB + (unsigned)j * 1024u));
        } else if (w == 5 || (w == 6 && u32i < 24)) {
          const rsrc_t r2 = __builtin_amdgcn_make_buffer_rsrc((void*)P.w2, 0, F_ * D_ * 2, 0x00020000);
          const int ks = u32i + (w - 5) * 32;
#pragma unroll
          for (int j = 0; j < 4; ++j) dma16(r2, raw_voff<2>(j, lane, 1024u, (unsigned)cg * 128u, 64u), rfl((unsigned)ks * 32u * 1024u), rfl(ldsb + L_PRE + (10 + (w - 5) * 4) * KB + (unsigned)j * 1024u));
        }
        unsigned biasv = 0;
        if (w < 2) { if (p < NP1) biasv = P.b1[p * 128 + w * 64 + lane]; } else if (w == 2) biasv = P.b2[cg * 64 + lane];
        u32x4 xr[16];
        {
          const GAS char* xp = (const GAS char*)(P.x + (size_t)(16 * tauA + tok) * D_ + 8 * g);
#pragma unroll
          for (int s = 0; s < 16; ++s) xr[s] = *(const GAS u32x4*)(xp + ((s + rotA) & 15) * 64);
          const GAS u32x2* rp = (const GAS u32x2*)(P.x + (size_t)(16 * tauB + tok) * D_ + 32 * ksB + 4 * g);
          const u32x2 a = rp[0], c = rp[4];
          resid = u32x4{a.x, a.y, c.x, c.y};
        }
        STAMP(23);
        asm volatile("s_waitcnt vmcnt(18)" ::: "memory");
        asm volatile("" : "+v"(epoch_raw), "+v"(biasv));
        tag = ((((u64)rfl(epoch_raw.y)) << 32) | rfl(epoch_raw.x)) + 1ull;
        if (w < 3) *(LAS float*)(ldsw + (w < 2 ? T_B1 + (w * 64 + lane) * 4 : T_B2 + lane * 4)) = bf2f(biasv);
        BAR();
        STAMP(1);
        if (w < 6) st128(__builtin_bit_cast(u32x4, raw_frag<3, 0>(lds + L_PRE + 18 * KB, w, lane)), rws, (unsigned)lane * 16u, rfl(WS_WQT + (unsigned)((p * 16 + t) * 6 + w) * 1024u));
        if (p < NP1) st128(__builtin_bit_cast(u32x4, raw_frag<4, 0>(lds + L_PRE, w, lane)), rws, (unsigned)lane * 16u, rfl(WS_W1T + (unsigned)((p * 16 + t) * 8 + w) * 1024u));
        if (w >= 6) st128(__builtin_bit_cast(u32x4, raw_frag<1, 1>(lds + L_PRE + 8 * KB, w - 6, lane)), rws, (unsigned)lane * 16u, rfl(WS_WOT + (unsigned)((cg * 16 + (u32i >> 1)) * 4 + (u32i & 1) * 2 + (w - 6)) * 1024u));
        if (w < 4 || u32i < 24) { const int hs = w >> 2, ks = u32i + hs * 32;
          st128(__builtin_bit_cast(u32x4, raw_frag<2, 0>(lds + L_PRE + (10 + hs * 4) * KB, w & 3, lane)), rws, (unsigned)lane * 16u, rfl(WS_W2T + (unsigned)((cg * 56 + ks) * 4 + (w & 3)) * 1024u)); }
        VMWAIT0();
        BAR();
        if (w == 0 && lane == 0) set_flag(rws, FL_WT + (unsigned)(t * 16 + p) * 128u, tag);
        STAMP(2);
        {
          float s = 0.f;
#pragma unroll
          for (int i = 0; i < 16; ++i) { const u32x4 v = xr[i];
            s += (bflo(v.x) + bfhi(v.x)) + (bflo(v.y) + bfhi(v.y)) + (bflo(v.z) + bfhi(v.z)) + (bflo(v.w) + bfhi(v.w)); }
          s = quad_sum(s); mean = s * (1.0f / 512.0f);
          float m2 = 0.f;
#pragma unroll
          for (int i = 0; i < 16; ++i) { const u32x4 v = xr[i];
#pragma unroll
            for (int e = 0; e < 4; ++e) { const float a = bflo(v[e]) - mean, c = bfhi(v[e]) - mean; m2 += a * a + c * c; } }
          m2 = quad_sum(m2); var = m2 * (1.0f / 512.0f);
#pragma unroll
          for (int i = 0; i < 16; ++i) {
            auto a0 = __builtin_amdgcn_permlane16_swap(xr[i].x, xr[i].z, false, false), a1 = __builtin_amdgcn_permlane16_swap(xr[i].y, xr[i].w, false, false);
            auto b0 = __builtin_amdgcn_permlane32_swap(a0[0], a0[1], false, false), b1 = __builtin_amdgcn_permlane32_swap(a1[0], a1[1], false, false);
            X[i] = asfrag(u32x4{b0[0], b1[0], b0[1], b1[1]});
          }
        }
        if (w == 0) {
          const int grp = lane >> 4, pg = grp == 0 ? p : 8 * (cg >> 2) + 4 * (grp - 1) + (cg & 3);
          poll_flags(rws, FL_WT + (unsigned)(((lane & 15) * 16 + pg) * 128), 48, tag, lane, dead);
        }
        BAR();
        STAMP(24);
        for (int i = w; i < 96; i += 8) { const int sl = i / 6, ct = i - 6 * sl;
          dma16_sc1(rws, (unsigned)lane * 16u, rfl(WS_WQT + (unsigned)((p * 16 + ((sl + rotA) & 15)) * 6 + ct) * 1024u), rfl(ldsb + MAIN + (unsigned)i * 1024u)); }
        VMWAIT0();
        BAR();
        STAMP(27);
        if (w < 6) {
          f32x4 cs = {0.f, 0.f, 0.f, 0.f};
#pragma unroll
          for (int s = 0; s < 16; ++s) cs = mfma16(ldsfrag(lds + MAIN + (s * 6 + w) * KB + lane * 16), ones_frag(), cs);
          if (tok == 0) *(LAS f32x4*)(ldsw + T_CSQ + (w * 16 + 4 * g) * 4) = cs;
        }
        BAR();
      }
      auto touch_o1 = [&]() INL {
        const int tq = t & 3;
        if (w < 4) { if (p < NP1) touch8k(rws, (unsigned)lane * 128u, rfl(WS_W1T + (unsigned)(p * 128 + tq * 32 + w * 8) * 1024u), ldsb + T_DUMP); }
        else if (w == 4) touch8k(rws, (unsigned)lane * 128u, rfl(WS_WOT + (unsigned)(cg * 64 + (tq * 2 + hh) * 8) * 1024u), ldsb + T_DUMP);
      };
      STAMP(16 + 32 * L);
      if constexpr (L == 0) {
        touch_o1();
        gemm_lds<4, 0, 16, 16, 0>(akv, X, lds + MAIN + 2 * KB + lane * 16, noload, [](int s) INL { return s * 6 * KB; }, nohook);
      } else {
        u32x2 pv[4];
        stats_load(rws, rfl(WS_ST2 + (unsigned)tauA * 2048u), lane, pv);
        auto xl = [&](int s) INL { return asfrag(ld128(rws, (unsigned)lane * 16u, rfl(WS_X2 + (unsigned)(tauA * 16 + ((s + rotA) & 15)) * 1024u))); };
#pragma unroll
        for (int s = 0; s < 6; ++s) X[s] = xl(s);
        stats_from_partials(pv, mean, var);
        gemm_lds<4, 0, 16, 16, 6>(akv, X, lds + MAIN + 2 * KB + lane * 16, xl, [](int s) INL { return s * 6 * KB; }, nohook);
        touch_o1();
      }
      const float rs = __builtin_amdgcn_rsqf(var + EPS), rm = rs * mean;
      const int plane = (g & 1) * 2 + (g >> 1);
      const int tl = 16 * w + tok;
      auto qkv_pack = [&](const f32x4& a0, const f32x4& a1, int c0, float sc) INL {
        const f32x4 cs0 = *(const LAS f32x4*)(lds + T_CSQ + (c0 * 16 + 4 * g) * 4), cs1 = *(const LAS f32x4*)(lds + T_CSQ + (c0 * 16 + 16 + 4 * g) * 4);
        float v[8];
#pragma unroll
        for (int j = 0; j < 4; ++j) { v[j] = (rs * a0[j] - rm * cs0[j]) * sc; v[4 + j] = (rs * a1[j] - rm * cs1[j]) * sc; }
        auto s0 = __builtin_amdgcn_permlane16_swap(cvtpk(v[0], v[1]), cvtpk(v[4], v[5]), false, false);
        auto s1 = __builtin_amdgcn_permlane16_swap(cvtpk(v[2], v[3]), cvtpk(v[6], v[7]), false, false);
        return u32x4{s0[0], s1[0], s0[1], s1[1]};
      };
      {
        const u32x4 ko = qkv_pack(akv[0], akv[1], 2, 1.0f), vo = qkv_pack(akv[2], akv[3], 4, 1.0f);
        st128(ko, rws, (unsigned)(plane * 2048 + tl * 16), rfl(WS_KG + (unsigned)((L * 16 + t) * 16 + p) * 8192u));
        st128(vo, rws, (unsigned)(plane * 2048 + tl * 16), rfl(WS_VG + (unsigned)((L * 16 + t) * 16 + p) * 8192u));
        *(LAS u32x4*)(ldsw + A_K + plane * PLN + (256 + tl) * 16) = ko;
        *(LAS u32x4*)(ldsw + A_V + plane * PLN + (256 + tl) * 16) = vo;
      }
      STAMP(3 + 32 * L);
      f32x4 aq[2] = {f32x4{0.f, 0.f, 0.f, 0.f}, f32x4{0.f, 0.f, 0.f, 0.f}};
      gemm_lds<2, 0, 8, 16, 0>(aq, X, lds + MAIN + lane * 16, noload, [](int s) INL { return s * 6 * KB; }, nohook);
      VMWAIT0();
      BAR();
      if (w == 0 && lane == 0) set_flag(rws, flag_row(L, K_KV) + (unsigned)(t * 16 + p) * 128u, tag);
      STAMP(4 + 32 * L);
      gemm_lds<2, 8, 16, 16, 0>(aq, X, lds + MAIN + lane * 16, noload, [](int s) INL { return s * 6 * KB; }, nohook);
      const u32x4 qo = qkv_pack(aq[0], aq[1], 0, QSCALE);
      if (w == 0) poll_flags(rws, flag_row(L, K_KV) + (unsigned)(((t - 1 - lane) * 16 + p) * 128), t < 2 ? t : 2, tag, lane, dead);
      BAR();
      STAMP(5 + 32 * L);
      {
        const int d = 1 + (w >> 2);
        u32x4 hv[4];
        if (t - d >= 0) {
#pragma unroll
          for (int i = 0; i < 4; ++i) { const int pc = (w & 3) * 4 + i, kv = pc >> 3, pl = (pc >> 1) & 3, hf = pc & 1;
            hv[i] = ld128(rws, (unsigned)lane * 16u, rfl((kv ? WS_VG : WS_KG) + (unsigned)((L * 16 + t - d) * 16 + p) * 8192u + (unsigned)(pl * 2048 + hf * 1024))); }
        }
        *(LAS u32x4*)(ldsw + A_Q + plane * 2048 + tl * 16) = qo;
        if (w * 64 + lane < 400) *(LAS u32x4*)(ldsw + A_ONE + (w * 64 + lane) * 16) = __builtin_bit_cast(u32x4, ones_frag());
        if (t - d >= 0) {
#pragma unroll
          for (int i = 0; i < 4; ++i) { const int pc = (w & 3) * 4 + i, kv = pc >> 3, pl = (pc >> 1) & 3, hf = pc & 1;
            *(LAS u32x4*)(ldsw + (kv ? A_V : A_K) + pl * PLN + ((2 - d) * 128 + hf * 64 + lane) * 16) = hv[i]; }
        }
      }
      BAR();
      STAMP(6 + 32 * L);
    }
    FRESH();
    {
      const int hd = w & 1, qi = w >> 1, ql = lane & 31, h = lane >> 5;
      const int rotA = 2 * (p & 7);
      if (w >= 4) touch8k(rws, (unsigned)lane * 128u, rfl(WS_W2T + (unsigned)(cg * 224 + ((t & 3) * 2 + hh) * 28 + (w - 4) * 8) * 1024u), ldsb + T_DUMP);
      auto hook = [&](int j) INL {
        if (p < NP1) { const int i = j * 8 + w, sl = j;
          dma16_sc1(rws, (unsigned)lane * 16u, rfl(WS_W1T + (unsigned)((p * 16 + ((sl + rotA) & 15)) * 8 + w) * 1024u), rfl(ldsb + L_W1 + (unsigned)i * 1024u)); }
      };
      STAMP(17 + 32 * L);
      u32x4 ov;
      if (t >= 2) ov = attn_wave<true>(lds, hd, qi, 0, lane, hook); else ov = attn_wave<false>(lds, hd, qi, 8 - 4 * t - qi, lane, hook);
      asm volatile("" :: "v"(ov));
      STAMP(18 + 32 * L);
      const int tq = 8 * t + 2 * qi;
      st128(ov, rws, (unsigned)((ql >> 4) * 16384 + ((2 * hd + h) * 16 + (ql & 15)) * 16), rfl(WS_OG + (unsigned)((L * 128 + tq) * 16 + p) * 1024u));
      VMWAIT0();
      BAR();
      if (w == 0 && lane == 0) set_flag(rws, flag_row(L, K_O) + (unsigned)(t * 16 + p) * 128u, tag);
      STAMP(7 + 32 * L);
      const int rotO = 2 * (p & 3);
      if (w > 0) for (int i = w - 1; i < 64; i += 7) { const int sl = i >> 2, ks = (sl & 8) | ((sl + rotO) & 7);
        dma16_sc1(rws, (unsigned)lane * 16u, rfl(WS_WOT + (unsigned)((cg * 16 + ks) * 4 + (i & 3)) * 1024u), rfl(ldsb + L_WO + (unsigned)i * 1024u)); }
      if (w == 0) poll_flags(rws, flag_row(L, K_O) + (unsigned)(t * 16 + lane) * 128u, 16, tag, lane, dead);
      VMWAIT0();
      BAR();
      STAMP(8 + 32 * L);
    }
    FRESH();
    {
      const int rotO = 2 * (p & 3), rotA = 2 * (p & 7);
      bf16x8 Xo[8];
#pragma unroll
      for (int i = 0; i < 8; ++i) Xo[i] = asfrag(ld128(rws, (unsigned)lane * 16u, rfl(WS_OG + (unsigned)((L * 128 + tauB) * 16 + 8 * kh + ((i + rotO) & 7)) * 1024u)));
      f32x4 acc[4];
#pragma unroll
      for (int c = 0; c < 4; ++c) acc[c] = f32x4{0.f, 0.f, 0.f, 0.f};
      gemm_lds<4, 0, 8, 8, 0>(acc, Xo, lds + L_WO + kh * 32 * KB + lane * 16, noload, [](int s) INL { return s * 4 * KB; }, nohook);
      STAMP(19 + 32 * L);
      *(LAS f32x4*)(ldsw + MAIN + (w * 2 + 0) * KB + lane * 16) = kh ? acc[0] : acc[2];
      *(LAS f32x4*)(ldsw + MAIN + (w * 2 + 1) * KB + lane * 16) = kh ? acc[1] : acc[3];
      BAR();
      const f32x4 o0 = *(const LAS f32x4*)(lds + MAIN + ((w ^ 4) * 2 + 0) * KB + lane * 16), o1 = *(const LAS f32x4*)(lds + MAIN + ((w ^ 4) * 2 + 1) * KB + lane * 16);
      const f32x4 k0 = kh ? acc[2] : acc[0], k1 = kh ? acc[3] : acc[1];
      float v[8];
      v[0] = k0[0] + o0[0] + bflo(resid.x); v[1] = k0[1] + o0[1] + bfhi(resid.x); v[2] = k0[2] + o0[2] + bflo(resid.y); v[3] = k0[3] + o0[3] + bfhi(resid.y);
      v[4] = k1[0] + o1[0] + bflo(resid.z); v[5] = k1[1] + o1[1] + bfhi(resid.z); v[6] = k1[2] + o1[2] + bflo(resid.w); v[7] = k1[3] + o1[3] + bfhi(resid.w);
      publish_x(rws, v, rfl(WS_X1 + (unsigned)((L * 128 + tauB) * 16 + ksB) * 1024u), rfl(WS_ST1 + (unsigned)((L * 128 + tauB) * 16 + ksB) * 128u), lane, resid);
      VMWAIT0();
      BAR();
      if (w == 0 && lane == 0) set_flag(rws, flag_row(L, K_X1) + (unsigned)(t * 16 + p) * 128u, tag);
      STAMP(9 + 32 * L);
      if (p < NP1 && w > 0) for (int i = 72 + w - 1; i < 128; i += 7) { const int sl = i >> 3;
        dma16_sc1(rws, (unsigned)lane * 16u, rfl(WS_W1T + (unsigned)((p * 16 + ((sl + rotA) & 15)) * 8 + (i & 7)) * 1024u), rfl(ldsb + L_W1 + (unsigned)i * 1024u)); }
      if (w == 0) poll_flags(rws, flag_row(L, K_X1) + (unsigned)(t * 16 + lane) * 128u, 16, tag, lane, dead);
      VMWAIT0();
      BAR();
      STAMP(10 + 32 * L);
    }
    auto dma_w2 = [&](int i0, int i1, int w0, int nw) INL {
      const int rot2 = 7 * (p & 3);
      if (w >= w0) for (int q = w - w0; q < (i1 - i0) * 8; q += nw) { const int i = i0 + (q >> 3), e = q & 7, slot = i >= 19 ? i - 19 : i; int k = i + rot2; k = k >= 28 ? k - 28 : k;
        dma16_sc1(rws, (unsigned)lane * 16u, rfl(WS_W2T + (unsigned)((cg * 56 + (e >> 2) * 28 + k) * 4 + (e & 3)) * 1024u), rfl(ldsb + MAIN + (unsigned)(slot * 8 + e) * 1024u)); }
    };
    FRESH();
    if (p < NP1) {
      const int rotA = 2 * (p & 7);
      bf16x8 X1[16];
      u32x2 pv1[4];
      stats_load(rws, rfl(WS_ST1 + (unsigned)(L * 128 + tauA) * 2048u), lane, pv1);
      auto xl = [&](int s) INL { return asfrag(ld128(rws, (unsigned)lane * 16u, rfl(WS_X1 + (unsigned)((L * 128 + tauA) * 16 + ((s + rotA) & 15)) * 1024u))); };
#pragma unroll
      for (int s = 0; s < 6; ++s) X1[s] = xl(s);
      float mean1, var1;
      stats_from_partials(pv1, mean1, var1);
      STAMP(26 + 32 * L);
      if constexpr (L == 0) {
        f32x4 cs = {0.f, 0.f, 0.f, 0.f};
#pragma unroll
        for (int s = 0; s < 16; ++s) cs = mfma16(ldsfrag(lds + L_W1 + (s * 8 + w) * KB + lane * 16), ones_frag(), cs);
        if (tok == 0) *(LAS f32x4*)(ldsw + T_CS1 + (w * 16 + 4 * g) * 4) = cs;
      }
      f32x4 acc[8];
#pragma unroll
      for (int c = 0; c < 8; ++c) acc[c] = f32x4{0.f, 0.f, 0.f, 0.f};
      auto hook = [&](int s) INL {
        if (s == 11 || s == 12) { const int i = s - 11, rot2 = 7 * (p & 3); int k = i + rot2; k = k >= 28 ? k - 28 : k;
          dma16_sc1(rws, (unsigned)lane * 16u, rfl(WS_W2T + (unsigned)((cg * 56 + (w >> 2) * 28 + k) * 4 + (w & 3)) * 1024u), rfl(ldsb + MAIN + (unsigned)(i * 8 + w) * 1024u)); }
        if (L == 0 && s == 13 && w < 3) touch8k(rws, (unsigned)lane * 128u, rfl(WS_WQT + (unsigned)(p * 96 + (t & 3) * 24 + w * 8) * 1024u), ldsb + T_DUMP);
      };
      gemm_lds<8, 0, 16, 16, 6>(acc, X1, lds + L_W1 + lane * 16, xl, [](int s) INL { return s * 8 * KB; }, hook);
      BAR();
      STAMP(11 + 32 * L);
      const float r1 = __builtin_amdgcn_rsqf(var1 + EPS), r2 = __builtin_amdgcn_rsqf(var1 * r1 * r1 + EPS), rs = r1 * r2, rm = rs * mean1;
      constexpr float C1 = -2.0f * 0.7978845608028654f * 1.4426950408889634f, C2 = C1 * 0.044715f;
      const f32x2 rs2 = {rs, rs}, nrm2 = {-rm, -rm}, c1v = {C1, C1}, c2v = {C2, C2}, one2 = {1.0f, 1.0f};
#pragma unroll
      for (int u = 0; u < 4; ++u) {
        f32x2 a[4], e[4];
#pragma unroll
        for (int hf = 0; hf < 2; ++hf) {
          const int c = 2 * u + hf;
          const f32x4 cs = *(const LAS f32x4*)(lds + T_CS1 + (c * 16 + 4 * g) * 4), bb = *(const LAS f32x4*)(lds + T_B1 + (c * 16 + 4 * g) * 4);
          a[2 * hf] = rs2 * f32x2{acc[c][0], acc[c][1]} + (nrm2 * f32x2{cs[0], cs[1]} + f32x2{bb[0], bb[1]});
          a[2 * hf + 1] = rs2 * f32x2{acc[c][2], acc[c][3]} + (nrm2 * f32x2{cs[2], cs[3]} + f32x2{bb[2], bb[3]});
        }
#pragma unroll
        for (int i = 0; i < 4; ++i) e[i] = a[i] * (c2v * (a[i] * a[i]) + c1v);
#pragma unroll
        for (int i = 0; i < 4; ++i) e[i] = f32x2{__builtin_amdgcn_exp2f(e[i].x), __builtin_amdgcn_exp2f(e[i].y)} + one2;
#pragma unroll
        for (int i = 0; i < 4; ++i) e[i] = a[i] * f32x2{__builtin_amdgcn_rcpf(e[i].x), __builtin_amdgcn_rcpf(e[i].y)};
        st128(u32x4{cvtpk2(e[0]), cvtpk2(e[1]), cvtpk2(e[2]), cvtpk2(e[3])}, rws, (unsigned)lane * 16u, rfl(WS_HG + (unsigned)((L * 128 + tauA) * 56 + 4 * p + u) * 1024u));
      }
      STAMP(20 + 32 * L);
      VMWAIT0();
      BAR();
      if (w == 0 && lane == 0) set_flag(rws, flag_row(L, K_H) + (unsigned)(t * 16 + p) * 128u, tag);
      STAMP(12 + 32 * L);
      dma_w2(2, 19, 1, 7);
    } else {
      if (L == 1 && b == 255 && w == 0) {
        bool d2 = dead;
#pragma unroll 1
        for (int i = 0; i < 4; ++i) poll_flags(rws, FL_WT + (unsigned)(i * 64 + lane) * 128u, 64, tag, lane, d2);
        if (lane == 0) set_flag(rws, WS_EPOCH, tag);
      }
      dma_w2(0, 19, 1, 7);
    }
    FRESH();
    {
      STAMP(30 + 32 * L);
      if (w == 0) poll_flags(rws, flag_row(L, K_H) + (unsigned)(t * 16 + lane) * 128u, NP1, tag, lane, dead);
      VMWAIT0();
      BAR();
      STAMP(13 + 32 * L);
      const int rot2 = 7 * (p & 3);
      bf16x8 Xh[28];
      auto xl = [&](int i) INL { int k = i + rot2; k = k >= 28 ? k - 28 : k; return asfrag(ld128(rws, (unsigned)lane * 16u, rfl(WS_HG + (unsigned)((L * 128 + tauB) * 56 + 28 * kh + k) * 1024u))); };
#pragma unroll
      for (int i = 0; i < 8; ++i) Xh[i] = xl(i);
      f32x4 acc[4];
#pragma unroll
      for (int c = 0; c < 4; ++c) acc[c] = f32x4{0.f, 0.f, 0.f, 0.f};
      gemm_lds<4, 0, 9, 28, 8>(acc, Xh, lds + MAIN + kh * 4 * KB + lane * 16, xl, [](int s) INL { return s * 8 * KB; }, nohook);
      STAMP(21 + 32 * L);
      BAR();
      dma_w2(19, 28, 0, 8);
      gemm_lds<4, 9, 19, 28, 8>(acc, Xh, lds + MAIN + kh * 4 * KB + lane * 16, xl, [](int s) INL { return s * 8 * KB; }, nohook);
      VMWAIT0();
      BAR();
      STAMP(22 + 32 * L);
      gemm_lds<4, 19, 28, 28, 8>(acc, Xh, lds + MAIN + kh * 4 * KB + lane * 16, xl, [](int s) INL { return (s - 19) * 8 * KB; }, nohook);
      STAMP(14 + 32 * L);
      *(LAS f32x4*)(ldsw + L_RED2 + (w * 2 + 0) * KB + lane * 16) = kh ? acc[0] : acc[2];
      *(LAS f32x4*)(ldsw + L_RED2 + (w * 2 + 1) * KB + lane * 16) = kh ? acc[1] : acc[3];
      BAR();
      const f32x4 o0 = *(const LAS f32x4*)(lds + L_RED2 + ((w ^ 4) * 2 + 0) * KB + lane * 16), o1 = *(const LAS f32x4*)(lds + L_RED2 + ((w ^ 4) * 2 + 1) * KB + lane * 16);
      const f32x4 k0 = kh ? acc[2] : acc[0], k1 = kh ? acc[3] : acc[1];
      const f32x4 b0 = *(const LAS f32x4*)(lds + T_B2 + (kh * 32 + 4 * g) * 4), b1v = *(const LAS f32x4*)(lds + T_B2 + (kh * 32 + 16 + 4 * g) * 4);
      float v[8];
      v[0] = k0[0] + o0[0] + b0[0] + bflo(resid.x); v[1] = k0[1] + o0[1] + b0[1] + bfhi(resid.x); v[2] = k0[2] + o0[2] + b0[2] + bflo(resid.y); v[3] = k0[3] + o0[3] + b0[3] + bfhi(resid.y);
      v[4] = k1[0] + o1[0] + b1v[0] + bflo(resid.z); v[5] = k1[1] + o1[1] + b1v[1] + bfhi(resid.z); v[6] = k1[2] + o1[2] + b1v[2] + bflo(resid.w); v[7] = k1[3] + o1[3] + b1v[3] + bfhi(resid.w);
      if constexpr (L == 1) {
        const unsigned a0 = cvtpk(v[0], v[1]), a1 = cvtpk(v[2], v[3]), c0 = cvtpk(v[4], v[5]), c1 = cvtpk(v[6], v[7]);
        auto s0 = __builtin_amdgcn_permlane16_swap(a0, c0, false, false);
        auto s1 = __builtin_amdgcn_permlane16_swap(a1, c1, false, false);
        const u32x4 o = {s0[0], s1[0], s0[1], s1[1]};
        const int choff = (g & 1) * 16 + (g >> 1) * 8;
        *(GAS u32x4*)(P.out + (size_t)(16 * tauB + tok) * D_ + 32 * ksB + choff) = o;
        STAMP(15 + 32 * L); RT(60); STAMP_OUT();
      } else {
        publish_x(rws, v, rfl(WS_X2 + (unsigned)(tauB * 16 + ksB) * 1024u), rfl(WS_ST2 + (unsigned)(tauB * 16 + ksB) * 128u), lane, resid);
        VMWAIT0();
        BAR();
        if (w == 0 && lane == 0) set_flag(rws, flag_row(0, K_X2) + (unsigned)(t * 16 + p) * 128u, tag);
        const int rotA = 2 * (p & 7);
        if (w > 0) for (int i = w - 1; i < 96; i += 7) { const int sl = i / 6, ct = i - 6 * sl;
          dma16_sc1(rws, (unsigned)lane * 16u, rfl(WS_WQT + (unsigned)((p * 16 + ((sl + rotA) & 15)) * 6 + ct) * 1024u), rfl(ldsb + MAIN + (unsigned)i * 1024u)); }
        if (w == 0) poll_flags(rws, flag_row(0, K_X2) + (unsigned)(t * 16 + lane) * 128u, 16, tag, lane, dead);
        VMWAIT0();
        BAR();
        STAMP(15 + 32 * L);
      }
    }
  };
  layer(IC<0>{});
  layer(IC<1>{});
}

extern "C" void kernel_launch(void* const* d_in, const int* in_sizes, int n_in, void* d_out, int out_size, void* d_ws, size_t ws_size, hipStream_t stream) {
  Params P;
  P.x = (const u16*)d_in[0]; P.wqkv = (const u16*)d_in[1]; P.wo = (const u16*)d_in[2];
  P.w1 = (const u16*)d_in[4]; P.b1 = (const u16*)d_in[5]; P.w2 = (const u16*)d_in[6]; P.b2 = (const u16*)d_in[7];
  P.out = (u16*)d_out; P.ws = (unsigned char*)d_ws;
  mega<<<256, 512, 0, stream>>>(P);
}
```

```cpp
#include <hip/hip_runtime.h>
#include <stdint.h>

typedef unsigned short u16;
typedef unsigned long long u64;
typedef short bf16x8 __attribute__((ext_vector_type(8)));
typedef short s16x4 __attribute__((ext_vector_type(4)));
typedef float f32x2 __attribute__((ext_vector_type(2)));
typedef float f32x4 __attribute__((ext_vector_type(4)));
typedef float f32x16 __attribute__((ext_vector_type(16)));
typedef unsigned u32x4 __attribute__((ext_vector_type(4)));
typedef unsigned u32x2 __attribute__((ext_vector_type(2)));
typedef __bf16 bf16x2_t __attribute__((ext_vector_type(2)));
#define LAS __attribute__((address_space(3)))
#define GAS __attribute__((address_space(1)))
#define DI __device__ __forceinline__
#define INL __attribute__((always_inline))
typedef __amdgpu_buffer_rsrc_t rsrc_t;

constexpr int S_ = 2048, D_ = 512, F_ = 1792;
constexpr float EPS = 1e-5f;
constexpr float QSCALE = 0.25f * 1.4426950408889634f;
constexpr int NP1 = 14;

constexpr unsigned WS_EPOCH = 0, WS_ERR = 128, WS_FLAGS = 4096;
enum { K_KV = 0, K_O = 1, K_X1 = 2, K_H = 3, K_X2 = 4 };
constexpr unsigned flag_row(int L, int kind) { return WS_FLAGS + (unsigned)((L * 5 + kind) * 256 * 128); }
constexpr unsigned FL_WT = flag_row(1, K_X2);
constexpr unsigned WS_WQT = 0x60000;
constexpr unsigned WS_WOT = WS_WQT + 16 * 96 * 1024;
constexpr unsigned WS_W1T = WS_WOT + 8 * 64 * 1024;
constexpr unsigned WS_W2T = WS_W1T + NP1 * 128 * 1024;
constexpr unsigned WS_KG = WS_W2T + 8 * 224 * 1024;
constexpr unsigned WS_VG = WS_KG + 2 * 256 * 8192;
constexpr unsigned WS_OG = WS_VG + 2 * 256 * 8192;
constexpr unsigned WS_X1 = WS_OG + 2 * 128 * 16 * 1024;
constexpr unsigned WS_ST1 = WS_X1 + 2 * 128 * 16 * 1024;
constexpr unsigned WS_HG = WS_ST1 + 2 * 128 * 16 * 128;
constexpr unsigned WS_X2 = WS_HG + 2 * 128 * 56 * 1024;
constexpr unsigned WS_ST2 = WS_X2 + 128 * 16 * 1024;
constexpr unsigned WS_DBG = WS_ST2 + 128 * 16 * 128;
constexpr unsigned WS_END = WS_DBG + 256 * 8 * 128 * 8;

constexpr int KB = 1024;
constexpr int LDS_BYTES = 163840;
constexpr int T_CSQ = 0;
constexpr int T_CS1 = 512;
constexpr int T_B1 = 1024;
constexpr int T_B2 = 1536;
constexpr int T_DUMP = 1792;
constexpr int MAIN = 2048;
constexpr int PLN = 384 * 16 + 64;
constexpr int A_K = MAIN + 96 * KB, A_V = A_K + 4 * PLN, A_Q = MAIN, A_ONE = A_Q + 4 * 2048;
static_assert(A_V + 4 * PLN <= LDS_BYTES && A_ONE + 6400 <= MAIN + 16 * KB, "attention buffers");
constexpr int L_W1 = MAIN + 30 * KB;
constexpr int L_WO = MAIN + 16 * KB;
constexpr int L_PRE = MAIN + 96 * KB;
constexpr int L_RED2 = MAIN + 96 * KB;
static_assert(L_W1 + 128 * KB <= LDS_BYTES && L_WO + 64 * KB <= LDS_BYTES, "images");

template <int V> struct IC { static constexpr int value = V; };
struct Params {
  const u16 *x, *wqkv, *wo, *w1, *b1, *w2, *b2;
  u16* out;
  unsigned char* ws;
};

DI float bf2f(unsigned v) { return __uint_as_float(v << 16); }
DI float bflo(unsigned v) { return __uint_as_float(v << 16); }
DI float bfhi(unsigned v) { return __uint_as_float(v & 0xffff0000u); }
DI unsigned cvtpk(float lo, float hi) { f32x2 v = {lo, hi}; bf16x2_t b = __builtin_convertvector(v, bf16x2_t); return __builtin_bit_cast(unsigned, b); }
DI unsigned cvtpk2(f32x2 v) { bf16x2_t b = __builtin_convertvector(v, bf16x2_t); return __builtin_bit_cast(unsigned, b); }
DI f32x4 mfma16(bf16x8 a, bf16x8 b, f32x4 c) { return __builtin_amdgcn_mfma_f32_16x16x32_bf16(a, b, c, 0, 0, 0); }
DI f32x16 mfma32(bf16x8 a, bf16x8 b, f32x16 c) { return __builtin_amdgcn_mfma_f32_32x32x16_bf16(a, b, c, 0, 0, 0); }
DI bf16x8 ones_frag() { unsigned o = 0x3F803F80u; asm volatile("" : "+v"(o)); return __builtin_bit_cast(bf16x8, u32x4{o, o, o, o}); }
DI bf16x8 asfrag(u32x4 v) { return __builtin_bit_cast(bf16x8, v); }
DI s16x4 trd(const LAS char* p) { return __builtin_bit_cast(s16x4, __builtin_amdgcn_ds_read_tr16_b64_v4i16((LAS s16x4*)p)); }
DI bf16x8 ldsfrag(const LAS char* p) { return *(const LAS bf16x8*)p; }
DI float max3f(float a, float b, float c) { return __builtin_fmaxf(__builtin_fmaxf(a, b), c); }
DI float swap32_add(float x) { auto r = __builtin_amdgcn_permlane32_swap(__float_as_uint(x), __float_as_uint(x), false, false); return __uint_as_float(r[0]) + __uint_as_float(r[1]); }
DI float swap16_add(float x) { auto r = __builtin_amdgcn_permlane16_swap(__float_as_uint(x), __float_as_uint(x), false, false); return __uint_as_float(r[0]) + __uint_as_float(r[1]); }
DI float quad_sum(float x) { return swap32_add(swap16_add(x)); }
DI float swap32_max(float x) { auto r = __builtin_amdgcn_permlane32_swap(__float_as_uint(x), __float_as_uint(x), false, false); return fmaxf(__uint_as_float(r[0]), __uint_as_float(r[1])); }

#define FRESH() do { asm volatile("" : "+v"(lane), "+s"(w), "+s"(t), "+s"(p)); tok = lane & 15; g = lane >> 4; hh = (p >> 2) & 1; cg = 4 * (p >> 3) + (p & 3); tt = w & 3; kh = w >> 2; \
  tauA = 8 * t + w; tauB = 8 * t + 4 * hh + tt; ksB = 2 * cg + kh; u32i = 2 * t + hh; } while (0)
#define PLANE ((g & 1) * 2 + (g >> 1))
#define TL (16 * w + tok)
#define FRESH_V() do { asm volatile("" : "+v"(lane)); tok = lane & 15; g = lane >> 4; } while (0)
#define BAR() asm volatile("s_waitcnt lgkmcnt(0)\n\ts_barrier" ::: "memory")
#define VMWAIT0() asm volatile("s_waitcnt vmcnt(0)" ::: "memory")
#define SBAR() __builtin_amdgcn_sched_barrier(0)
constexpr int SC1 = 16;
#define XAUX 16
DI u32x4 ld128(rsrc_t r, unsigned voff, unsigned soff) { return __builtin_amdgcn_raw_buffer_load_b128(r, voff, soff, XAUX); }
DI u32x2 ld64(rsrc_t r, unsigned voff, unsigned soff) { return __builtin_amdgcn_raw_buffer_load_b64(r, voff, soff, SC1); }
DI void st128(u32x4 v, rsrc_t r, unsigned voff, unsigned soff) { __builtin_amdgcn_raw_buffer_store_b128(v, r, voff + soff, 0, SC1); }
DI void st64(u32x2 v, rsrc_t r, unsigned voff, unsigned soff) { __builtin_amdgcn_raw_buffer_store_b64(v, r, voff + soff, 0, SC1); }
DI unsigned rfl(unsigned x) { return __builtin_amdgcn_readfirstlane(x); }
DI void dma16(rsrc_t r, unsigned voff, unsigned soff, unsigned ldsaddr) {
  asm volatile("s_mov_b32 m0, %0\n\ts_nop 3\n\tbuffer_load_dwordx4 %1, %2, %3 offen lds" :: "s"(rfl(ldsaddr)), "v"(voff), "s"(r), "s"(rfl(soff)) : "memory");
}
DI void dma16_sc1(rsrc_t r, unsigned voff, unsigned soff, unsigned ldsaddr) {
  asm volatile("s_mov_b32 m0, %0\n\ts_nop 3\n\tbuffer_load_dwordx4 %1, %2, %3 offen sc1 lds" :: "s"(rfl(ldsaddr)), "v"(voff), "s"(r), "s"(rfl(soff)) : "memory");
}
DI void dma4k(rsrc_t r, unsigned voff, unsigned soff, unsigned ldsaddr) {
  asm volatile("s_mov_b32 m0, %0\n\ts_nop 3\n\tbuffer_load_dwordx4 %1, %2, %3 offen sc1 lds\n\tbuffer_load_dwordx4 %1, %2, %3 offen offset:1024 sc1 lds\n\t"
               "buffer_load_dwordx4 %1, %2, %3 offen offset:2048 sc1 lds\n\tbuffer_load_dwordx4 %1, %2, %3 offen offset:3072 sc1 lds" :: "s"(rfl(ldsaddr)), "v"(voff), "s"(r), "s"(rfl(soff)) : "memory");
}
DI void dma2k(rsrc_t r, unsigned voff, unsigned soff, unsigned ldsaddr) {
  asm volatile("s_mov_b32 m0, %0\n\ts_nop 3\n\tbuffer_load_dwordx4 %1, %2, %3 offen sc1 lds\n\tbuffer_load_dwordx4 %1, %2, %3 offen offset:1024 sc1 lds" :: "s"(rfl(ldsaddr)), "v"(voff), "s"(r), "s"(rfl(soff)) : "memory");
}
DI void touch8k(rsrc_t r, unsigned voff, unsigned soff, unsigned ldsdump) {
  asm volatile("s_mov_b32 m0, %0\n\ts_nop 3\n\tbuffer_load_dword %1, %2, %3 offen sc1 lds" :: "s"(rfl(ldsdump)), "v"(voff), "s"(r), "s"(rfl(soff)) : "memory");
}

constexpr unsigned SENT = 0xFFFFFFFFu;
constexpr bool NOSYNC_ = false;
DI unsigned san(unsigned v) { return v < SENT - 1u ? v : SENT - 1u; }
DI u32x4 san4(u32x4 v) { return u32x4{san(v.x), san(v.y), san(v.z), san(v.w)}; }
DI unsigned umax(unsigned a, unsigned b) { return a > b ? a : b; }
DI bool bad4(u32x4 v) {
  return __any(umax(umax(v.x, v.y), umax(v.z, v.w)) == SENT); }
DI bool bad2(u32x2 v) {
  return __any(umax(v.x, v.y) == SENT); }
constexpr unsigned SPIN_CAP = 1u << 14;
DI void poll_flags(rsrc_t rws, unsigned voff, int n, u64 tag, int lane, bool& dead) {
  if (dead) return;
  for (unsigned spins = 0;;) {
    u32x2 v = {(unsigned)tag, (unsigned)(tag >> 32)};
    if (lane < n) v = ld64(rws, voff, 0);
    const bool ok = v.x == (unsigned)tag && v.y == (unsigned)(tag >> 32);
    if (__all(ok)) break;
    __builtin_amdgcn_s_sleep(1);
    if (++spins > SPIN_CAP) { dead = true; if (lane == 0) __builtin_amdgcn_raw_buffer_store_b32(1u, rws, WS_ERR, 0, SC1); break; }
  }
}
template <class XL> DI void ensure(bf16x8& x, int s, XL xload, rsrc_t rws, bool& dead) {
  if (__builtin_expect(bad4(__builtin_bit_cast(u32x4, x)), 0)) {
    unsigned spins = 0;
    while (!dead) {
      __builtin_amdgcn_s_sleep(2);
      x = xload(s);
      if (!bad4(__builtin_bit_cast(u32x4, x))) break;
      if (++spins > SPIN_CAP) { dead = true; __builtin_amdgcn_raw_buffer_store_b32(1u, rws, WS_ERR, 0, SC1); }
    }
    __builtin_amdgcn_s_waitcnt(0x0F70);
  }
}
DI void set_flag(rsrc_t rws, unsigned off, u64 tag) { u32x2 v = {(unsigned)tag, (unsigned)(tag >> 32)}; st64(v, rws, off, 0); }

template <int NP> DI unsigned raw_voff(int j, int lane, unsigned rowpitch, unsigned cb0, unsigned cbs) {
  const int beta = 2 * j + (lane >> 5), oct = beta / NP, pair = beta % NP, r = (lane >> 2) & 7, q4 = lane & 3;
  return (unsigned)(8 * oct + r) * rowpitch + cb0 + (unsigned)pair * cbs + (unsigned)((((q4 >> 1) ^ (r >> 2) ^ oct) & 1) * 32 + (q4 & 1) * 16);
}
template <int NP, int ORD> DI bf16x8 raw_frag(const LAS char* kbase, int ct, int lane) {
  const int g = lane >> 4, q = (lane & 15) >> 2, pp = lane & 3, pair = ct >> 1, c = ct & 1;
  int o1, r1, o2, r2;
  if (ORD == 0) { o1 = g >> 1; r1 = 4 * (g & 1) + q; o2 = o1 + 2; r2 = r1; }
  else { o1 = 2 * (g >> 1); r1 = 4 * (g & 1) + q; o2 = o1 + 1; r2 = r1; }
  const s16x4 lo = trd(kbase + (o1 * NP + pair) * 512 + r1 * 64 + ((c ^ (r1 >> 2) ^ o1) & 1) * 32 + pp * 8);
  const s16x4 hi = trd(kbase + (o2 * NP + pair) * 512 + r2 * 64 + ((c ^ (r2 >> 2) ^ o2) & 1) * 32 + pp * 8);
  return __builtin_shufflevector(lo, hi, 0, 1, 2, 3, 4, 5, 6, 7);
}

template <int NT, int S0, int S1, int NX, int PD, bool VAL, class XL, class OFF, class HOOK>
DI void gemm_lds(f32x4 (&acc)[NT], bf16x8 (&X)[NX], const LAS char* wb, XL xload, OFF off, HOOK hook, rsrc_t rws, bool& dead) {
  bf16x8 wf[2][NT];
#pragma unroll
  for (int c = 0; c < NT; ++c) wf[0][c] = ldsfrag(wb + off(S0) + c * 1024);
  if (VAL && S0 == 0) ensure(X[0], 0, xload, rws, dead);
  SBAR();
#pragma unroll
  for (int s = S0; s < S1; ++s) {
    const int cur = (s - S0) & 1;
    if (PD > 0) {
#pragma unroll
      for (int e = 0; e < 2; ++e) { const int ix = PD + 2 * s + e; if (ix < NX) X[ix] = xload(ix); }
    }
    if (VAL && s + 1 < NX) ensure(X[s + 1], s + 1, xload, rws, dead);
    hook(s);
    __builtin_amdgcn_s_waitcnt(0xC07F);
    SBAR();
#pragma unroll
    for (int c = 0; c < NT; ++c) {
      if (s + 1 < S1) wf[cur ^ 1][c] = ldsfrag(wb + off(s + 1) + c * 1024);
      acc[c] = mfma16(wf[cur][c], X[s], acc[c]);
    }
#pragma unroll
    for (int c = 0; c < NT; ++c) {
      __builtin_amdgcn_sched_group_barrier(0x008, 1, 0);
      if (s + 1 < S1 && 2 * c < NT) __builtin_amdgcn_sched_group_barrier(0x100, 2, 0);
    }
    SBAR();
  }
}

#define STAMP(i) do {} while (0)
#define RT(i) do {} while (0)
#define STAMP_OUT() do {} while (0)

DI void stats_load(rsrc_t rws, unsigned base, int lane, u32x2 (&pv)[4]) {
  const int tok = lane & 15, g = lane >> 4;
#pragma unroll
  for (int i = 0; i < 4; ++i) pv[i] = ld64(rws, (unsigned)((4 * g + i) * 128 + tok * 8), base);
}
DI void stats_ensure(rsrc_t rws, unsigned base, int lane, u32x2 (&pv)[4], bool& dead) {
  if (__builtin_expect(__any(umax(umax(umax(pv[0].x, pv[0].y), umax(pv[1].x, pv[1].y)), umax(umax(pv[2].x, pv[2].y), umax(pv[3].x, pv[3].y))) == SENT) && !NOSYNC_, 0)) {
    unsigned spins = 0;
    while (!dead) {
      __builtin_amdgcn_s_sleep(2);
      stats_load(rws, base, lane, pv);
      if (!(bad2(pv[0]) || bad2(pv[1]) || bad2(pv[2]) || bad2(pv[3]))) break;
      if (++spins > SPIN_CAP) { dead = true; __builtin_amdgcn_raw_buffer_store_b32(1u, rws, WS_ERR, 0, SC1); }
    }
    __builtin_amdgcn_s_waitcnt(0x0F70);
  }
}
DI void stats_from_partials(const u32x2 (&pv)[4], float& mean, float& var) {
  float s = 0.f;
#pragma unroll
  for (int i = 0; i < 4; ++i) s += __uint_as_float(pv[i].x);
  s = quad_sum(s);
  mean = s * (1.0f / 512.0f);
  float m2 = 0.f;
#pragma unroll
  for (int i = 0; i < 4; ++i) { const float d = __uint_as_float(pv[i].x) * (1.0f / 32.0f) - mean; m2 += __uint_as_float(pv[i].y) + 32.0f * d * d; }
  m2 = quad_sum(m2);
  var = m2 * (1.0f / 512.0f);
}

DI void publish_x(rsrc_t rws, const float (&v)[8], unsigned xoff, unsigned stoff, int lane, u32x4& packed) {
  packed.x = cvtpk(v[0], v[1]); packed.y = cvtpk(v[2], v[3]); packed.z = cvtpk(v[4], v[5]); packed.w = cvtpk(v[6], v[7]);
  st128(packed, rws, (unsigned)lane * 16u, xoff);
  float r[8] = {bflo(packed.x), bfhi(packed.x), bflo(packed.y), bfhi(packed.y), bflo(packed.z), bfhi(packed.z), bflo(packed.w), bfhi(packed.w)};
  float s = 0.f;
#pragma unroll
  for (int i = 0; i < 8; ++i) s += r[i];
  s = quad_sum(s);
  const float mu = s * (1.0f / 32.0f);
  float m2 = 0.f;
#pragma unroll
  for (int i = 0; i < 8; ++i) { const float d = r[i] - mu; m2 += d * d; }
  m2 = quad_sum(m2);
  if (lane < 16) { u32x2 o = {__float_as_uint(s), __float_as_uint(m2)}; st64(o, rws, (unsigned)lane * 8u, stoff); }
}

template <bool FULL, class HOOK>
DI u32x4 attn_wave(const LAS char* lds, int hd, int qi, int jmin, int lane, HOOK hook) {
  const int ql = lane & 31, h = lane >> 5;
  const bf16x8 Q = ldsfrag(lds + A_Q + (hd * 2 + h) * 2048 + (32 * qi + ql) * 16);
  const LAS char* kb = lds + A_K + (hd * 2 + h) * PLN + (32 * qi + ql) * 16;
  const int G = lane >> 4, q4 = (lane & 15) >> 2, pp = lane & 3;
  const LAS char* vb = lds + ((G & 1) ? A_ONE : A_V + (hd * 2 + (pp >> 1)) * PLN) + (32 * qi + 4 * h + q4) * 16 + (pp & 1) * 8;
  constexpr float NEG = -1e30f;
  const f32x16 zero16 = {0.f,0.f,0.f,0.f,0.f,0.f,0.f,0.f,0.f,0.f,0.f,0.f,0.f,0.f,0.f,0.f};
  f32x16 biasD;
  const int dq = ql - 4 * h;
#pragma unroll
  for (int r = 0; r < 16; ++r) biasD[r] = ((r & 3) + 8 * (r >> 2) <= dq) ? 0.f : NEG;
  float m0 = NEG, m1 = NEG;
  {
    bf16x8 ka = ldsfrag(kb + 8 * 512), kn = ka;
    if (FULL || 7 >= jmin) kn = ldsfrag(kb + 7 * 512);
    f32x16 sa = mfma32(ka, Q, biasD), sb = sa;
    SBAR();
#pragma unroll
    for (int j = 8; j >= 0; --j) {
      f32x16& cur = (j & 1) ? sb : sa; f32x16& nxt = (j & 1) ? sa : sb;
      if (j >= 1 && (FULL || j - 1 >= jmin)) {
        const bf16x8 kj = kn;
        if (j >= 2 && (FULL || j - 2 >= jmin)) kn = ldsfrag(kb + (j - 2) * 512);
        if (j - 1 == 0) { f32x16 bf;
#pragma unroll
          for (int r = 0; r < 16; ++r) bf[r] = NEG - biasD[r];
          nxt = mfma32(kj, Q, bf); }
        else nxt = mfma32(kj, Q, zero16);
      }
      if (FULL || j >= jmin) {
#pragma unroll
        for (int r = 0; r < 16; r += 4) { m0 = max3f(m0, cur[r], cur[r + 1]); m1 = max3f(m1, cur[r + 2], cur[r + 3]); }
        asm volatile("" : "+v"(m0), "+v"(m1));
      }
      SBAR();
    }
  }
  const float m = swap32_max(fmaxf(m0, m1));
  f32x16 negm;
#pragma unroll
  for (int r = 0; r < 16; ++r) negm[r] = -m;
  f32x16 o = zero16;
  {
    bf16x8 ka = ldsfrag(kb + 8 * 512), kn = ka;
    if (FULL || 7 >= jmin) kn = ldsfrag(kb + 7 * 512);
    f32x16 sa = mfma32(ka, Q, biasD + negm), sb = sa;
    SBAR();
#pragma unroll
    for (int j = 8; j >= 0; --j) {
      f32x16& cur = (j & 1) ? sb : sa; f32x16& nxt = (j & 1) ? sa : sb;
      const bool have = FULL || j >= jmin;
      hook(j);
      s16x4 v0 = {}, v1 = {}, v2 = {}, v3 = {};
      if (have) { v0 = trd(vb + (32 * j) * 16); v1 = trd(vb + (32 * j + 8) * 16); v2 = trd(vb + (32 * j + 16) * 16); v3 = trd(vb + (32 * j + 24) * 16); }
      if (j >= 1 && (FULL || j - 1 >= jmin)) {
        const bf16x8 kj = kn;
        if (j >= 2 && (FULL || j - 2 >= jmin)) kn = ldsfrag(kb + (j - 2) * 512);
        if (j - 1 == 0) { f32x16 cf;
#pragma unroll
          for (int r = 0; r < 16; ++r) cf[r] = (NEG - biasD[r]) - m;
          nxt = mfma32(kj, Q, cf); }
        else nxt = mfma32(kj, Q, negm);
      }
      if (have) {
        unsigned pk[8];
#pragma unroll
        for (int r = 0; r < 8; ++r) pk[r] = cvtpk(__builtin_amdgcn_exp2f(cur[2 * r]), __builtin_amdgcn_exp2f(cur[2 * r + 1]));
        o = mfma32(__builtin_shufflevector(v0, v1, 0, 1, 2, 3, 4, 5, 6, 7), asfrag(u32x4{pk[0], pk[1], pk[2], pk[3]}), o);
        o = mfma32(__builtin_shufflevector(v2, v3, 0, 1, 2, 3, 4, 5, 6, 7), asfrag(u32x4{pk[4], pk[5], pk[6], pk[7]}), o);
      }
      SBAR();
    }
  }
  const float inv = __builtin_amdgcn_rcpf(o[8]);
  return u32x4{cvtpk(o[0] * inv, o[1] * inv), cvtpk(o[2] * inv, o[3] * inv), cvtpk(o[4] * inv, o[5] * inv), cvtpk(o[6] * inv, o[7] * inv)};
}

__global__ __launch_bounds__(512, 2) void mega(Params P) {
  __shared__ __attribute__((aligned(16))) char lds_[LDS_BYTES];
  const LAS char* lds = (const LAS char*)lds_;
  LAS char* ldsw = (LAS char*)lds_;
  const unsigned ldsb = (unsigned)(size_t)lds_;
  int w = rfl(threadIdx.x >> 6);
  int lane = (int)__builtin_amdgcn_mbcnt_hi(~0u, __builtin_amdgcn_mbcnt_lo(~0u, 0u));
  const int b = blockIdx.x;
  int t = 4 * ((b & 7) >> 1) + (b >> 6), p = 8 * (b & 1) + ((b >> 3) & 7);
  int hh = (p >> 2) & 1, cg = 4 * (p >> 3) + (p & 3);
  int tt = w & 3, kh = w >> 2;
  int tauA = 8 * t + w, tauB = 8 * t + 4 * hh + tt, ksB = 2 * cg + kh, u32i = 2 * t + hh;
  int tok = lane & 15, g = lane >> 4;
  const rsrc_t rws = __builtin_amdgcn_make_buffer_rsrc(P.ws, 0, (int)WS_END, 0x00020000);
  bool dead = false;
  STAMP(0); RT(59);
  u64 tag = 0;
  u32x4 resid = {0u, 0u, 0u, 0u};
  auto nohook = [](int) INL {};
  auto noload = [](int) INL { return bf16x8{}; };

  int nL = 2;
  asm volatile("" : "+s"(nL));
#pragma unroll 1
  for (int L = 0; L < nL; ++L) {
    asm volatile("" : "+s"(L));
    const unsigned v16 = (unsigned)lane * 16u;
    auto load_wq = [&](int w0, int nw) INL {
      if (w >= w0) for (int sl = w - w0; sl < 16; sl += nw) { const unsigned src = WS_WQT + (unsigned)((p * 16 + (sl ^ (2 * (p & 7)))) * 6) * 1024u, dst = ldsb + MAIN + (unsigned)sl * 6144u;
        dma4k(rws, (unsigned)lane * 16u, src, dst); dma2k(rws, (unsigned)lane * 16u, src + 4096u, dst + 4096u); }
    };
    auto load_w1 = [&](int h0, int h1, int w0, int nw) INL {
      if (p < NP1 && w >= w0) for (int hs = h0 + w - w0; hs < h1; hs += nw) { const int sl = hs >> 1;
        dma4k(rws, (unsigned)lane * 16u, WS_W1T + (unsigned)((p * 16 + (sl ^ (2 * (p & 7)))) * 8 + (hs & 1) * 4) * 1024u, ldsb + L_W1 + (unsigned)hs * 4096u); }
    };
    auto w2_half = [&](int i, int e) INL {
      const int slot = i >= 19 ? i - 19 : i, k = i ^ (p & 3);
      dma4k(rws, (unsigned)lane * 16u, WS_W2T + (unsigned)((cg * 56 + e * 28 + k) * 4) * 1024u, ldsb + MAIN + (unsigned)(slot * 8 + e * 4) * 1024u);
    };
    auto load_w2 = [&](int i0, int i1, int w0, int nw) INL {
      if (w >= w0) for (int q = 2 * i0 + w - w0; q < 2 * i1; q += nw) w2_half(q >> 1, q & 1);
    };
    (void)v16;
    FRESH();
    {
      bf16x8 X[16];
      float mean = 0.f, var = 0.f;
      const int rotA = 2 * (p & 7);
      f32x4 akv[4];
#pragma unroll
      for (int c = 0; c < 4; ++c) akv[c] = f32x4{0.f, 0.f, 0.f, 0.f};
      if (L == 0) {
        const rsrc_t rx = __builtin_amdgcn_make_buffer_rsrc((void*)P.x, 0, S_ * D_ * 2, 0x00020000);
        if (w < 2) {
          const rsrc_t rq = __builtin_amdgcn_make_buffer_rsrc((void*)P.wqkv, 0, D_ * 3 * D_ * 2, 0x00020000);
#pragma unroll
          for (int jj = 0; jj < 3; ++jj) { const int j = 3 * w + jj; dma16(rq, raw_voff<3>(j, lane, 3072u, (unsigned)p * 64u, 1024u), rfl((unsigned)t * 32u * 3072u), rfl(ldsb + L_PRE + 18 * KB + (unsigned)j * 1024u)); }
        } else if (w < 4) {
          touch8k(rx, (unsigned)lane * 128u, rfl((unsigned)(128 * t + 16 * (p & 7)) * 1024u + (unsigned)(w - 2) * 8192u), ldsb + T_DUMP);
          if (p < NP1) {
            const rsrc_t r1 = __builtin_amdgcn_make_buffer_rsrc((void*)P.w1, 0, D_ * F_ * 2, 0x00020000);
#pragma unroll
            for (int jj = 0; jj < 4; ++jj) { const int j = 4 * (w - 2) + jj; dma16(r1, raw_voff<4>(j, lane, 3584u, (unsigned)p * 256u, 64u), rfl((unsigned)t * 32u * 3584u), rfl(ldsb + L_PRE + (unsigned)j * 1024u)); }
          }
        } else if (w == 4) {
          const rsrc_t ro = __builtin_amdgcn_make_buffer_rsrc((void*)P.wo, 0, D_ * D_ * 2, 0x00020000);
#pragma unroll
          for (int j = 0; j < 2; ++j) dma16(ro, raw_voff<1>(j, lane, 1024u, (unsigned)(cg * 128 + (u32i & 1) * 64), 0u), rfl((unsigned)(u32i >> 1) * 32u * 1024u), rfl(ldsb + L_PRE + 8 * KB + (unsigned)j * 1024u));
        } else if (w == 5 || (w == 6 && u32i < 24)) {
          const rsrc_t r2 = __builtin_amdgcn_make_buffer_rsrc((void*)P.w2, 0, F_ * D_ * 2, 0x00020000);
          const int ks = u32i + (w - 5) * 32;
#pragma unroll
          for (int j = 0; j < 4; ++j) dma16(r2, raw_voff<2>(j, lane, 1024u, (unsigned)cg * 128u, 64u), rfl((unsigned)ks * 32u * 1024u), rfl(ldsb + L_PRE + (10 + (w - 5) * 4) * KB + (unsigned)j * 1024u));
        }
        u32x2 epoch_raw = ld64(rws, WS_EPOCH, 0);
        STAMP(23);
        VMWAIT0();
        tag = ((((u64)rfl(epoch_raw.y)) << 32) | rfl(epoch_raw.x)) + 1ull;
        BAR();
        STAMP(1);
        if (w < 6) st128(__builtin_bit_cast(u32x4, raw_frag<3, 0>(lds + L_PRE + 18 * KB, w, lane)), rws, (unsigned)lane * 16u, rfl(WS_WQT + (unsigned)((p * 16 + t) * 6 + w) * 1024u));
        VMWAIT0();
        BAR();
        if (w == 0 && lane == 0) set_flag(rws, FL_WT + (unsigned)(t * 16 + p) * 128u, tag);
        STAMP(2);
        FRESH_V();
        if (p < NP1) st128(__builtin_bit_cast(u32x4, raw_frag<4, 0>(lds + L_PRE, w, lane)), rws, (unsigned)lane * 16u, rfl(WS_W1T + (unsigned)((p * 16 + t) * 8 + w) * 1024u));
        if (w >= 6) st128(__builtin_bit_cast(u32x4, raw_frag<1, 1>(lds + L_PRE + 8 * KB, w - 6, lane)), rws, (unsigned)lane * 16u, rfl(WS_WOT + (unsigned)((cg * 16 + (u32i >> 1)) * 4 + (u32i & 1) * 2 + (w - 6)) * 1024u));
        if (w < 4 || u32i < 24) { const int hs = w >> 2, ks = u32i + hs * 32;
          st128(__builtin_bit_cast(u32x4, raw_frag<2, 0>(lds + L_PRE + (10 + hs * 4) * KB, w & 3, lane)), rws, (unsigned)lane * 16u, rfl(WS_W2T + (unsigned)((cg * 56 + ks) * 4 + (w & 3)) * 1024u)); }
        u32x4 xr[16];
        const unsigned xo = (unsigned)((16 * tauA + tok) * 1024 + g * 16);
        if (w == 0) poll_flags(rws, FL_WT + (unsigned)((lane * 16 + p) * 128), 16, tag, lane, dead);
#pragma unroll
        for (int s = 0; s < 16; ++s) xr[s] = __builtin_amdgcn_raw_buffer_load_b128(rx, xo, (unsigned)((s ^ rotA) * 64), 0);
        BAR();
        STAMP(24);
        load_wq(0, 8);
        unsigned biasv = 0;
        if (w < 2) { if (p < NP1) biasv = P.b1[p * 128 + w * 64 + lane]; } else if (w == 2) biasv = P.b2[cg * 64 + lane];
        {
          const GAS u32x2* rp = (const GAS u32x2*)(P.x + (size_t)(16 * tauB + tok) * D_ + 32 * ksB + 4 * g);
          const u32x2 r0 = rp[0], r1 = rp[4];
          resid = u32x4{r0.x, r0.y, r1.x, r1.y};
        }
        FRESH_V();
        {
          float s = 0.f;
#pragma unroll
          for (int i = 0; i < 16; ++i) { const u32x4 v = xr[i];
            s += (bflo(v.x) + bfhi(v.x)) + (bflo(v.y) + bfhi(v.y)) + (bflo(v.z) + bfhi(v.z)) + (bflo(v.w) + bfhi(v.w)); asm volatile("" : "+v"(s)); SBAR(); }
          s = quad_sum(s); mean = s * (1.0f / 512.0f);
          float m2 = 0.f;
#pragma unroll
          for (int i = 0; i < 16; ++i) asm volatile("" : "+v"(xr[i]));
#pragma unroll
          for (int i = 0; i < 16; ++i) { const u32x4 v = xr[i];
#pragma unroll
            for (int e = 0; e < 4; ++e) { const float x0 = bflo(v[e]) - mean, x1 = bfhi(v[e]) - mean; m2 += x0 * x0 + x1 * x1; } asm volatile("" : "+v"(m2)); SBAR(); }
          m2 = quad_sum(m2); var = m2 * (1.0f / 512.0f);
#pragma unroll
          for (int i = 0; i < 16; ++i) {
            auto a0 = __builtin_amdgcn_permlane16_swap(xr[i].x, xr[i].z, false, false), a1 = __builtin_amdgcn_permlane16_swap(xr[i].y, xr[i].w, false, false);
            auto b0 = __builtin_amdgcn_permlane32_swap(a0[0], a0[1], false, false), b1 = __builtin_amdgcn_permlane32_swap(a1[0], a1[1], false, false);
            X[i] = asfrag(u32x4{b0[0], b1[0], b0[1], b1[1]});
            SBAR();
          }
        }
        if (w < 3) *(LAS float*)(ldsw + (w < 2 ? T_B1 + (w * 64 + lane) * 4 : T_B2 + lane * 4)) = bf2f(biasv);
        VMWAIT0();
        BAR();
        STAMP(27);
        if (w < 6) {
          f32x4 cs = {0.f, 0.f, 0.f, 0.f};
#pragma unroll
          for (int s = 0; s < 16; ++s) cs = mfma16(ldsfrag(lds + MAIN + (s * 6 + w) * KB + lane * 16), ones_frag(), cs);
          if (tok == 0) *(LAS f32x4*)(ldsw + T_CSQ + (w * 16 + 4 * g) * 4) = cs;
        }
        STAMP(16 + 32 * L);
        gemm_lds<4, 0, 16, 16, 0, false>(akv, X, lds + MAIN + 2 * KB + lane * 16, noload, [](int s) INL { return s * 6 * KB; }, nohook, rws, dead);
        BAR();
      } else {
        STAMP(16 + 32 * L);
        u32x2 pv[4];
        stats_load(rws, WS_ST2 + (unsigned)tauA * 2048u, lane, pv);
        const unsigned xb = WS_X2 + (unsigned)(tauA * 16 + rotA) * 1024u;
        auto xl = [&](int s) INL { return asfrag(ld128(rws, (unsigned)lane * 16u, xb ^ ((unsigned)s << 10))); };
#pragma unroll
        for (int s = 0; s < 8; ++s) X[s] = xl(s);
        stats_from_partials(pv, mean, var);
        gemm_lds<4, 0, 16, 16, 8, false>(akv, X, lds + MAIN + 2 * KB + lane * 16, xl, [](int s) INL { return s * 6 * KB; }, nohook, rws, dead);
      }
      FRESH();
      const float rs = __builtin_amdgcn_rsqf(var + EPS), rm = rs * mean;
      auto qkv_pack = [&](const f32x4& a0, const f32x4& a1, int c0, float sc) INL {
        const f32x4 cs0 = *(const LAS f32x4*)(lds + T_CSQ + (c0 * 16 + 4 * g) * 4), cs1 = *(const LAS f32x4*)(lds + T_CSQ + (c0 * 16 + 16 + 4 * g) * 4);
        float v[8];
#pragma unroll
        for (int j = 0; j < 4; ++j) { v[j] = (rs * a0[j] - rm * cs0[j]) * sc; v[4 + j] = (rs * a1[j] - rm * cs1[j]) * sc; }
        auto s0 = __builtin_amdgcn_permlane16_swap(cvtpk(v[0], v[1]), cvtpk(v[4], v[5]), false, false);
        auto s1 = __builtin_amdgcn_permlane16_swap(cvtpk(v[2], v[3]), cvtpk(v[6], v[7]), false, false);
        return u32x4{s0[0], s1[0], s0[1], s1[1]};
      };
      {
        const u32x4 ko = qkv_pack(akv[0], akv[1], 2, 1.0f), vo = qkv_pack(akv[2], akv[3], 4, 1.0f);
        st128(ko, rws, (unsigned)(PLANE * 2048 + TL * 16), WS_KG + (unsigned)((L * 16 + t) * 16 + p) * 8192u);
        st128(vo, rws, (unsigned)(PLANE * 2048 + TL * 16), WS_VG + (unsigned)((L * 16 + t) * 16 + p) * 8192u);
        *(LAS u32x4*)(ldsw + A_K + PLANE * PLN + (256 + TL) * 16) = ko;
        *(LAS u32x4*)(ldsw + A_V + PLANE * PLN + (256 + TL) * 16) = vo;
      }
      STAMP(3 + 32 * L);
      FRESH();
      f32x4 aq[2] = {f32x4{0.f, 0.f, 0.f, 0.f}, f32x4{0.f, 0.f, 0.f, 0.f}};
      gemm_lds<2, 0, 8, 16, 0, false>(aq, X, lds + MAIN + lane * 16, noload, [](int s) INL { return s * 6 * KB; }, nohook, rws, dead);
      VMWAIT0();
      BAR();
      if (w == 0 && lane == 0) set_flag(rws, flag_row(0, K_KV) + (unsigned)(L * 5 * 256 + t * 16 + p) * 128u, tag);
      gemm_lds<2, 8, 16, 16, 0, false>(aq, X, lds + MAIN + lane * 16, noload, [](int s) INL { return s * 6 * KB; }, nohook, rws, dead);
      STAMP(4 + 32 * L);
      FRESH();
      const u32x4 qo = qkv_pack(aq[0], aq[1], 0, QSCALE);
      if (w == 0) {
        if (L == 0) {
          const int grp = lane >> 4, pg = grp == 0 ? p : 8 * (cg >> 2) + 4 * (grp - 1) + (cg & 3);
          poll_flags(rws, flag_row(0, K_KV) + (unsigned)(((lane & 15) * 16 + pg) * 128), 48, tag, lane, dead);
        } else poll_flags(rws, flag_row(1, K_KV) + (unsigned)(((t - 1 - lane) * 16 + p) * 128), t < 2 ? t : 2, tag, lane, dead);
      }
      BAR();
      STAMP(5 + 32 * L);
      FRESH();
      {
        const int d = 1 + (w >> 2);
        u32x4 hv[4] = {u32x4{}, u32x4{}, u32x4{}, u32x4{}};
        if (t - d >= 0) {
          const unsigned hb = ((w & 2) ? WS_VG : WS_KG) + (unsigned)((L * 16 + t - d) * 16 + p) * 8192u + (unsigned)(w & 1) * 4096u;
#pragma unroll
          for (int i = 0; i < 4; ++i) hv[i] = ld128(rws, (unsigned)lane * 16u, hb + (unsigned)i * 1024u);
        }
        *(LAS u32x4*)(ldsw + A_Q + PLANE * 2048 + TL * 16) = qo;
        if (w * 64 + lane < 400) *(LAS u32x4*)(ldsw + A_ONE + (w * 64 + lane) * 16) = __builtin_bit_cast(u32x4, ones_frag());
        if (t - d >= 0) {
#pragma unroll
          for (int i = 0; i < 4; ++i) *(LAS u32x4*)(ldsw + ((w & 2) ? A_V : A_K) + (2 * (w & 1) + (i >> 1)) * PLN + ((2 - d) * 128 + (i & 1) * 64 + lane) * 16) = hv[i];
        }
      }
      BAR();
      STAMP(6 + 32 * L);
    }
    FRESH();
    {
      if (w >= 4 && p < NP1) touch8k(rws, (unsigned)lane * 128u, WS_W1T + (unsigned)(p * 128 + (t & 3) * 32 + (w - 4) * 8) * 1024u, ldsb + T_DUMP);
      STAMP(17 + 32 * L);
      u32x4 ov;
      if (t >= 2) ov = attn_wave<true>(lds, w & 1, w >> 1, 0, lane, nohook); else ov = attn_wave<false>(lds, w & 1, w >> 1, 8 - 4 * t - (w >> 1), lane, nohook);
      FRESH();
      st128(ov, rws, (unsigned)(((lane & 31) >> 4) * 16384 + ((2 * (w & 1) + (lane >> 5)) * 16 + (lane & 15)) * 16), WS_OG + (unsigned)((L * 128 + 8 * t + 2 * (w >> 1)) * 16 + p) * 1024u);
      STAMP(18 + 32 * L);
      if (w < 4) {
#pragma unroll
        for (int i = 0; i < 4; ++i) { const int sl = w + 4 * i; dma4k(rws, (unsigned)lane * 16u, WS_WOT + (unsigned)((cg * 16 + (sl ^ (2 * (p & 3)))) * 4) * 1024u, ldsb + L_WO + (unsigned)sl * 4096u); }
      }
      VMWAIT0();
      BAR();
      if (w == 0 && lane == 0) set_flag(rws, flag_row(0, K_O) + (unsigned)(L * 5 * 256 + t * 16 + p) * 128u, tag);
      STAMP(7 + 32 * L);
      load_w1(14, 32, 1, 7);
      if (w == 0) poll_flags(rws, flag_row(0, K_O) + (unsigned)(L * 5 * 256 + t * 16 + lane) * 128u, 16, tag, lane, dead);
      BAR();
      STAMP(8 + 32 * L);
    }
    FRESH();
    {
      bf16x8 Xo[8];
      const unsigned xb = WS_OG + (unsigned)((L * 128 + tauB) * 16 + 8 * kh + 2 * (p & 3)) * 1024u;
      auto xl = [&](int i) INL { return asfrag(ld128(rws, (unsigned)lane * 16u, xb ^ ((unsigned)i << 10))); };
#pragma unroll
      for (int i = 0; i < 8; ++i) Xo[i] = xl(i);
      f32x4 acc[4];
#pragma unroll
      for (int c = 0; c < 4; ++c) acc[c] = f32x4{0.f, 0.f, 0.f, 0.f};
      gemm_lds<4, 0, 8, 8, 0, false>(acc, Xo, lds + L_WO + kh * 32 * KB + lane * 16, xl, [](int s) INL { return s * 4 * KB; }, nohook, rws, dead);
      STAMP(19 + 32 * L);
      FRESH();
      *(LAS f32x4*)(ldsw + MAIN + (w * 2 + 0) * KB + lane * 16) = kh ? acc[0] : acc[2];
      *(LAS f32x4*)(ldsw + MAIN + (w * 2 + 1) * KB + lane * 16) = kh ? acc[1] : acc[3];
      BAR();
      const f32x4 o0 = *(const LAS f32x4*)(lds + MAIN + ((w ^ 4) * 2 + 0) * KB + lane * 16), o1 = *(const LAS f32x4*)(lds + MAIN + ((w ^ 4) * 2 + 1) * KB + lane * 16);
      const f32x4 k0 = kh ? acc[2] : acc[0], k1 = kh ? acc[3] : acc[1];
      float v[8];
      v[0] = k0[0] + o0[0] + bflo(resid.x); v[1] = k0[1] + o0[1] + bfhi(resid.x); v[2] = k0[2] + o0[2] + bflo(resid.y); v[3] = k0[3] + o0[3] + bfhi(resid.y);
      v[4] = k1[0] + o1[0] + bflo(resid.z); v[5] = k1[1] + o1[1] + bfhi(resid.z); v[6] = k1[2] + o1[2] + bflo(resid.w); v[7] = k1[3] + o1[3] + bfhi(resid.w);
      publish_x(rws, v, WS_X1 + (unsigned)((L * 128 + tauB) * 16 + ksB) * 1024u, WS_ST1 + (unsigned)((L * 128 + tauB) * 16 + ksB) * 128u, lane, resid);
      VMWAIT0();
      BAR();
      if (w == 0 && lane == 0) set_flag(rws, flag_row(0, K_X1) + (unsigned)(L * 5 * 256 + t * 16 + p) * 128u, tag);
      STAMP(9 + 32 * L);
      load_w1(0, 14, 1, 7);
      if (w >= 4) touch8k(rws, (unsigned)lane * 128u, WS_W2T + (unsigned)(cg * 224 + ((t & 3) * 2 + hh) * 28 + (w - 4) * 8) * 1024u, ldsb + T_DUMP);
      if (w == 0) poll_flags(rws, flag_row(0, K_X1) + (unsigned)(L * 5 * 256 + t * 16 + lane) * 128u, 16, tag, lane, dead);
      if (w >= 4) asm volatile("s_waitcnt vmcnt(1)" ::: "memory"); else VMWAIT0();
      BAR();
      STAMP(10 + 32 * L);
    }
    FRESH();
    if (p < NP1) {
      bf16x8 X1[16];
      u32x2 pv1[4];
      stats_load(rws, WS_ST1 + (unsigned)(L * 128 + tauA) * 2048u, lane, pv1);
      const unsigned xb = WS_X1 + (unsigned)((L * 128 + tauA) * 16 + 2 * (p & 7)) * 1024u;
      auto xl = [&](int s) INL { return asfrag(ld128(rws, (unsigned)lane * 16u, xb ^ ((unsigned)s << 10))); };
#pragma unroll
      for (int s = 0; s < 8; ++s) X1[s] = xl(s);
      if (L == 0) {
        f32x4 cs = {0.f, 0.f, 0.f, 0.f};
#pragma unroll
        for (int s = 0; s < 16; ++s) cs = mfma16(ldsfrag(lds + L_W1 + (s * 8 + w) * KB + lane * 16), ones_frag(), cs);
        if (tok == 0) *(LAS f32x4*)(ldsw + T_CS1 + (w * 16 + 4 * g) * 4) = cs;
      }
      float mean1, var1;
      stats_from_partials(pv1, mean1, var1);
      STAMP(26 + 32 * L);
      f32x4 acc[8];
#pragma unroll
      for (int c = 0; c < 8; ++c) acc[c] = f32x4{0.f, 0.f, 0.f, 0.f};
      auto hook = [&](int s) INL {
        if (s == 13 && w < 6) w2_half(w >> 1, w & 1);
        if (s == 14 && L == 0 && w < 3) touch8k(rws, (unsigned)lane * 128u, WS_WQT + (unsigned)(p * 96 + (t & 3) * 24 + w * 8) * 1024u, ldsb + T_DUMP);
      };
      gemm_lds<8, 0, 16, 16, 8, false>(acc, X1, lds + L_W1 + lane * 16, xl, [](int s) INL { return s * 8 * KB; }, hook, rws, dead);
      BAR();
      STAMP(11 + 32 * L);
      FRESH_V();
      const float r1 = __builtin_amdgcn_rsqf(var1 + EPS), r2 = __builtin_amdgcn_rsqf(var1 * r1 * r1 + EPS), rs = r1 * r2, rm = rs * mean1;
      constexpr float C1 = -2.0f * 0.7978845608028654f * 1.4426950408889634f, C2 = C1 * 0.044715f;
      const f32x2 rs2 = {rs, rs}, nrm2 = {-rm, -rm}, c1v = {C1, C1}, c2v = {C2, C2}, one2 = {1.0f, 1.0f};
#pragma unroll
      for (int u = 0; u < 4; ++u) {
        f32x2 av[4], e[4];
#pragma unroll
        for (int hf = 0; hf < 2; ++hf) {
          const int c = 2 * u + hf;
          const f32x4 cs = *(const LAS f32x4*)(lds + T_CS1 + (c * 16 + 4 * g) * 4), bb = *(const LAS f32x4*)(lds + T_B1 + (c * 16 + 4 * g) * 4);
          av[2 * hf] = rs2 * f32x2{acc[c][0], acc[c][1]} + (nrm2 * f32x2{cs[0], cs[1]} + f32x2{bb[0], bb[1]});
          av[2 * hf + 1] = rs2 * f32x2{acc[c][2], acc[c][3]} + (nrm2 * f32x2{cs[2], cs[3]} + f32x2{bb[2], bb[3]});
        }
#pragma unroll
        for (int i = 0; i < 4; ++i) e[i] = av[i] * (c2v * (av[i] * av[i]) + c1v);
#pragma unroll
        for (int i = 0; i < 4; ++i) e[i] = f32x2{__builtin_amdgcn_exp2f(e[i].x), __builtin_amdgcn_exp2f(e[i].y)} + one2;
#pragma unroll
        for (int i = 0; i < 4; ++i) e[i] = av[i] * f32x2{__builtin_amdgcn_rcpf(e[i].x), __builtin_amdgcn_rcpf(e[i].y)};
        st128(u32x4{cvtpk2(e[0]), cvtpk2(e[1]), cvtpk2(e[2]), cvtpk2(e[3])}, rws, (unsigned)lane * 16u, WS_HG + (unsigned)((L * 128 + tauA) * 56 + 4 * p + u) * 1024u);
        SBAR();
        w2_half(3 + 4 * u + (w >> 1), w & 1);
        SBAR();
      }
      STAMP(20 + 32 * L);
      asm volatile("s_waitcnt vmcnt(4)" ::: "memory");
      BAR();
      if (w == 0 && lane == 0) set_flag(rws, flag_row(0, K_H) + (unsigned)(L * 5 * 256 + t * 16 + p) * 128u, tag);
    } else {
      if (L == 1 && b == 255 && w == 0) {
        bool d2 = dead;
#pragma unroll 1
        for (int i = 0; i < 4; ++i) poll_flags(rws, FL_WT + (unsigned)(i * 64 + lane) * 128u, 64, tag, lane, d2);
        if (lane == 0) set_flag(rws, WS_EPOCH, tag);
      }
      load_w2(0, 19, 0, 8);
    }
    FRESH();
    {
      STAMP(12 + 32 * L);
      if (w == 0) poll_flags(rws, flag_row(0, K_H) + (unsigned)(L * 5 * 256 + t * 16 + lane) * 128u, NP1, tag, lane, dead);
      VMWAIT0();
      BAR();
      STAMP(13 + 32 * L);
      bf16x8 Xh[28];
      const unsigned xb = WS_HG + (unsigned)((L * 128 + tauB) * 56 + 28 * kh + (p & 3)) * 1024u;
      auto xl = [&](int i) INL { return asfrag(ld128(rws, (unsigned)lane * 16u, (xb ^ ((unsigned)(i & 3) << 10)) + (unsigned)(i & ~3) * 1024u)); };
#pragma unroll
      for (int i = 0; i < 10; ++i) Xh[i] = xl(i);
      f32x4 acc[4];
#pragma unroll
      for (int c = 0; c < 4; ++c) acc[c] = f32x4{0.f, 0.f, 0.f, 0.f};
      gemm_lds<4, 0, 9, 28, 10, false>(acc, Xh, lds + MAIN + kh * 4 * KB + lane * 16, xl, [](int s) INL { return s * 8 * KB; }, nohook, rws, dead);
      STAMP(21 + 32 * L);
      BAR();
      load_w2(19, 28, 0, 8);
      gemm_lds<4, 9, 19, 28, 10, false>(acc, Xh, lds + MAIN + kh * 4 * KB + lane * 16, xl, [](int s) INL { return s * 8 * KB; }, nohook, rws, dead);
      VMWAIT0();
      BAR();
      STAMP(22 + 32 * L);
      gemm_lds<4, 19, 28, 28, 10, false>(acc, Xh, lds + MAIN + kh * 4 * KB + lane * 16, xl, [](int s) INL { return (s - 19) * 8 * KB; }, nohook, rws, dead);
      STAMP(14 + 32 * L);
      FRESH();
      *(LAS f32x4*)(ldsw + L_RED2 + (w * 2 + 0) * KB + lane * 16) = kh ? acc[0] : acc[2];
      *(LAS f32x4*)(ldsw + L_RED2 + (w * 2 + 1) * KB + lane * 16) = kh ? acc[1] : acc[3];
      BAR();
      const f32x4 o0 = *(const LAS f32x4*)(lds + L_RED2 + ((w ^ 4) * 2 + 0) * KB + lane * 16), o1 = *(const LAS f32x4*)(lds + L_RED2 + ((w ^ 4) * 2 + 1) * KB + lane * 16);
      const f32x4 k0 = kh ? acc[2] : acc[0], k1 = kh ? acc[3] : acc[1];
      const f32x4 b0 = *(const LAS f32x4*)(lds + T_B2 + (kh * 32 + 4 * g) * 4), b1v = *(const LAS f32x4*)(lds + T_B2 + (kh * 32 + 16 + 4 * g) * 4);
      float v[8];
      v[0] = k0[0] + o0[0] + b0[0] + bflo(resid.x); v[1] = k0[1] + o0[1] + b0[1] + bfhi(resid.x); v[2] = k0[2] + o0[2] + b0[2] + bflo(resid.y); v[3] = k0[3] + o0[3] + b0[3] + bfhi(resid.y);
      v[4] = k1[0] + o1[0] + b1v[0] + bflo(resid.z); v[5] = k1[1] + o1[1] + b1v[1] + bfhi(resid.z); v[6] = k1[2] + o1[2] + b1v[2] + bflo(resid.w); v[7] = k1[3] + o1[3] + b1v[3] + bfhi(resid.w);
      if (L == 1) {
        const unsigned a0 = cvtpk(v[0], v[1]), a1 = cvtpk(v[2], v[3]), c0 = cvtpk(v[4], v[5]), c1 = cvtpk(v[6], v[7]);
        auto s0 = __builtin_amdgcn_permlane16_swap(a0, c0, false, false);
        auto s1 = __builtin_amdgcn_permlane16_swap(a1, c1, false, false);
        const u32x4 o = {s0[0], s1[0], s0[1], s1[1]};
        const int choff = (g & 1) * 16 + (g >> 1) * 8;
        *(GAS u32x4*)(P.out + (size_t)(16 * tauB + tok) * D_ + 32 * ksB + choff) = o;
        STAMP(15 + 32 * L); RT(60); STAMP_OUT();
        break;
      }
      publish_x(rws, v, WS_X2 + (unsigned)(tauB * 16 + ksB) * 1024u, WS_ST2 + (unsigned)(tauB * 16 + ksB) * 128u, lane, resid);
      VMWAIT0();
      BAR();
      if (w == 0 && lane == 0) set_flag(rws, flag_row(0, K_X2) + (unsigned)(t * 16 + p) * 128u, tag);
      load_wq(1, 7);
      if (w == 0) poll_flags(rws, flag_row(0, K_X2) + (unsigned)(t * 16 + lane) * 128u, 16, tag, lane, dead);
      VMWAIT0();
      BAR();
      STAMP(15 + 32 * L);
    }
  }
}

extern "C" void kernel_launch(void* const* d_in, const int* in_sizes, int n_in, void* d_out, int out_size, void* d_ws, size_t ws_size, hipStream_t stream) {
  Params P;
  P.x = (const u16*)d_in[0]; P.wqkv = (const u16*)d_in[1]; P.wo = (const u16*)d_in[2];
  P.w1 = (const u16*)d_in[4]; P.b1 = (const u16*)d_in[5]; P.w2 = (const u16*)d_in[6]; P.b2 = (const u16*)d_in[7];
  P.out = (u16*)d_out; P.ws = (unsigned char*)d_ws;
  mega<<<256, 512, 0, stream>>>(P);
}
```

```cpp
#include <hip/hip_runtime.h>
#include <stdint.h>

typedef unsigned short u16;
typedef unsigned long long u64;
typedef short bf16x8 __attribute__((ext_vector_type(8)));
typedef short s16x4 __attribute__((ext_vector_type(4)));
typedef float f32x2 __attribute__((ext_vector_type(2)));
typedef float f32x4 __attribute__((ext_vector_type(4)));
typedef float f32x16 __attribute__((ext_vector_type(16)));
typedef unsigned u32x4 __attribute__((ext_vector_type(4)));
typedef unsigned u32x2 __attribute__((ext_vector_type(2)));
typedef __bf16 bf16x2_t __attribute__((ext_vector_type(2)));
#define LAS __attribute__((address_space(3)))
#define GAS __attribute__((address_space(1)))
#define DI __device__ __forceinline__
#define INL __attribute__((always_inline))
typedef __amdgpu_buffer_rsrc_t rsrc_t;

constexpr int S_ = 2048, D_ = 512, F_ = 1792;
constexpr float EPS = 1e-5f;
constexpr float QSCALE = 0.25f * 1.4426950408889634f;
constexpr int NP1 = 14;

constexpr unsigned WS_EPOCH = 0, WS_ERR = 128, WS_FLAGS = 4096;
enum { K_KV = 0, K_O = 1, K_X1 = 2, K_H = 3, K_X2 = 4 };
constexpr unsigned flag_row(int L, int kind) { return WS_FLAGS + (unsigned)((L * 5 + kind) * 256 * 128); }
constexpr unsigned FL_WT = flag_row(1, K_X2);
constexpr unsigned WS_WQT = 0x60000;
constexpr unsigned WS_WOT = WS_WQT + 16 * 96 * 1024;
constexpr unsigned WS_W1T = WS_WOT + 8 * 64 * 1024;
constexpr unsigned WS_W2T = WS_W1T + NP1 * 128 * 1024;
constexpr unsigned WS_KG = WS_W2T + 8 * 224 * 1024;
constexpr unsigned WS_VG = WS_KG + 2 * 256 * 8192;
constexpr unsigned WS_OG = WS_VG + 2 * 256 * 8192;
constexpr unsigned WS_X1 = WS_OG + 2 * 128 * 16 * 1024;
constexpr unsigned WS_ST1 = WS_X1 + 2 * 128 * 16 * 1024;
constexpr unsigned WS_HG = WS_ST1 + 2 * 128 * 16 * 128;
constexpr unsigned WS_X2 = WS_HG + 2 * 128 * 56 * 1024;
constexpr unsigned WS_ST2 = WS_X2 + 128 * 16 * 1024;
constexpr unsigned WS_DBG = WS_ST2 + 128 * 16 * 128;
constexpr unsigned WS_END = WS_DBG + 256 * 8 * 128 * 8;

constexpr int KB = 1024;
constexpr int CODE_BYTES = 40 * 1024;
constexpr int LDS_BYTES = 163840;
constexpr int T_CSQ = 0;
constexpr int T_CS1 = 512;
constexpr int T_B1 = 1024;
constexpr int T_B2 = 1536;
constexpr int T_DUMP = 1792;
constexpr int MAIN = 2048;
constexpr int PLN = 384 * 16 + 64;
constexpr int A_K = MAIN + 96 * KB, A_V = A_K + 4 * PLN, A_Q = MAIN, A_ONE = A_Q + 4 * 2048;
static_assert(A_V + 4 * PLN <= LDS_BYTES && A_ONE + 6400 <= MAIN + 16 * KB, "attention buffers");
constexpr int L_W1 = MAIN + 30 * KB;
constexpr int L_WO = MAIN + 16 * KB;
constexpr int L_PRE = MAIN + 96 * KB;
constexpr int L_RED2 = MAIN + 96 * KB;
static_assert(L_W1 + 128 * KB <= LDS_BYTES && L_WO + 64 * KB <= LDS_BYTES, "images");

template <int V> struct IC { static constexpr int value = V; };
struct Params {
  const u16 *x, *wqkv, *wo, *w1, *b1, *w2, *b2;
  u16* out;
  unsigned char* ws;
};

DI float bf2f(unsigned v) { return __uint_as_float(v << 16); }
DI float bflo(unsigned v) { return __uint_as_float(v << 16); }
DI float bfhi(unsigned v) { return __uint_as_float(v & 0xffff0000u); }
DI unsigned cvtpk(float lo, float hi) { f32x2 v = {lo, hi}; bf16x2_t b = __builtin_convertvector(v, bf16x2_t); return __builtin_bit_cast(unsigned, b); }
DI unsigned cvtpk2(f32x2 v) { bf16x2_t b = __builtin_convertvector(v, bf16x2_t); return __builtin_bit_cast(unsigned, b); }
DI f32x4 mfma16(bf16x8 a, bf16x8 b, f32x4 c) { return __builtin_amdgcn_mfma_f32_16x16x32_bf16(a, b, c, 0, 0, 0); }
DI f32x16 mfma32(bf16x8 a, bf16x8 b, f32x16 c) { return __builtin_amdgcn_mfma_f32_32x32x16_bf16(a, b, c, 0, 0, 0); }
DI bf16x8 ones_frag() { unsigned o = 0x3F803F80u; asm volatile("" : "+v"(o)); return __builtin_bit_cast(bf16x8, u32x4{o, o, o, o}); }
DI bf16x8 asfrag(u32x4 v) { return __builtin_bit_cast(bf16x8, v); }
DI s16x4 trd(const LAS char* p) { return __builtin_bit_cast(s16x4, __builtin_amdgcn_ds_read_tr16_b64_v4i16((LAS s16x4*)p)); }
DI bf16x8 ldsfrag(const LAS char* p) { return *(const LAS bf16x8*)p; }
DI float max3f(float a, float b, float c) { return __builtin_fmaxf(__builtin_fmaxf(a, b), c); }
DI float swap32_add(float x) { auto r = __builtin_amdgcn_permlane32_swap(__float_as_uint(x), __float_as_uint(x), false, false); return __uint_as_float(r[0]) + __uint_as_float(r[1]); }
DI float swap16_add(float x) { auto r = __builtin_amdgcn_permlane16_swap(__float_as_uint(x), __float_as_uint(x), false, false); return __uint_as_float(r[0]) + __uint_as_float(r[1]); }
DI float quad_sum(float x) { return swap32_add(swap16_add(x)); }
DI float swap32_max(float x) { auto r = __builtin_amdgcn_permlane32_swap(__float_as_uint(x), __float_as_uint(x), false, false); return fmaxf(__uint_as_float(r[0]), __uint_as_float(r[1])); }

#define FRESH() do { asm volatile("" : "+v"(lane), "+s"(w), "+s"(t), "+s"(p)); tok = lane & 15; g = lane >> 4; hh = (p >> 2) & 1; cg = 4 * (p >> 3) + (p & 3); tt = w & 3; kh = w >> 2; \
  tauA = 8 * t + w; tauB = 8 * t + 4 * hh + tt; ksB = 2 * cg + kh; u32i = 2 * t + hh; } while (0)
#define PLANE ((g & 1) * 2 + (g >> 1))
#define TL (16 * w + tok)
#define FRESH_V() do { asm volatile("" : "+v"(lane)); tok = lane & 15; g = lane >> 4; } while (0)
#define BAR() asm volatile("s_waitcnt lgkmcnt(0)\n\ts_barrier" ::: "memory")
#define VMWAIT0() asm volatile("s_waitcnt vmcnt(0)" ::: "memory")
#define SBAR() __builtin_amdgcn_sched_barrier(0)
constexpr int SC1 = 16;
#define XAUX 16
DI u32x4 ld128(rsrc_t r, unsigned voff, unsigned soff) { return __builtin_amdgcn_raw_buffer_load_b128(r, voff, soff, XAUX); }
DI u32x2 ld64(rsrc_t r, unsigned voff, unsigned soff) { return __builtin_amdgcn_raw_buffer_load_b64(r, voff, soff, SC1); }
DI void st128(u32x4 v, rsrc_t r, unsigned voff, unsigned soff) { __builtin_amdgcn_raw_buffer_store_b128(v, r, voff + soff, 0, SC1); }
DI void st64(u32x2 v, rsrc_t r, unsigned voff, unsigned soff) { __builtin_amdgcn_raw_buffer_store_b64(v, r, voff + soff, 0, SC1); }
DI unsigned rfl(unsigned x) { return __builtin_amdgcn_readfirstlane(x); }
DI void dma16(rsrc_t r, unsigned voff, unsigned soff, unsigned ldsaddr) {
  asm volatile("s_mov_b32 m0, %0\n\ts_nop 3\n\tbuffer_load_dwordx4 %1, %2, %3 offen lds" :: "s"(rfl(ldsaddr)), "v"(voff), "s"(r), "s"(rfl(soff)) : "memory");
}
DI void dma16_sc1(rsrc_t r, unsigned voff, unsigned soff, unsigned ldsaddr) {
  asm volatile("s_mov_b32 m0, %0\n\ts_nop 3\n\tbuffer_load_dwordx4 %1, %2, %3 offen sc1 lds" :: "s"(rfl(ldsaddr)), "v"(voff), "s"(r), "s"(rfl(soff)) : "memory");
}
DI void dma4k(rsrc_t r, unsigned voff, unsigned soff, unsigned ldsaddr) {
  asm volatile("s_mov_b32 m0, %0\n\ts_nop 3\n\tbuffer_load_dwordx4 %1, %2, %3 offen sc1 lds\n\tbuffer_load_dwordx4 %1, %2, %3 offen offset:1024 sc1 lds\n\t"
               "buffer_load_dwordx4 %1, %2, %3 offen offset:2048 sc1 lds\n\tbuffer_load_dwordx4 %1, %2, %3 offen offset:3072 sc1 lds" :: "s"(rfl(ldsaddr)), "v"(voff), "s"(r), "s"(rfl(soff)) : "memory");
}
DI void dma2k(rsrc_t r, unsigned voff, unsigned soff, unsigned ldsaddr) {
  asm volatile("s_mov_b32 m0, %0\n\ts_nop 3\n\tbuffer_load_dwordx4 %1, %2, %3 offen sc1 lds\n\tbuffer_load_dwordx4 %1, %2, %3 offen offset:1024 sc1 lds" :: "s"(rfl(ldsaddr)), "v"(voff), "s"(r), "s"(rfl(soff)) : "memory");
}
DI void touch8k(rsrc_t r, unsigned voff, unsigned soff, unsigned ldsdump) {
  asm volatile("s_mov_b32 m0, %0\n\ts_nop 3\n\tbuffer_load_dword %1, %2, %3 offen sc1 lds" :: "s"(rfl(ldsdump)), "v"(voff), "s"(r), "s"(rfl(soff)) : "memory");
}

constexpr unsigned SENT = 0xFFFFFFFFu;
constexpr bool NOSYNC_ = false;
DI unsigned san(unsigned v) { return v < SENT - 1u ? v : SENT - 1u; }
DI u32x4 san4(u32x4 v) { return u32x4{san(v.x), san(v.y), san(v.z), san(v.w)}; }
DI unsigned umax(unsigned a, unsigned b) { return a > b ? a : b; }
DI bool bad4(u32x4 v) {
  return __any(umax(umax(v.x, v.y), umax(v.z, v.w)) == SENT); }
DI bool bad2(u32x2 v) {
  return __any(umax(v.x, v.y) == SENT); }
constexpr unsigned SPIN_CAP = 1u << 14;
DI void poll_flags(rsrc_t rws, unsigned voff, int n, u64 tag, int lane, bool& dead) {
  if (dead) return;
  for (unsigned spins = 0;;) {
    u32x2 v = {(unsigned)tag, (unsigned)(tag >> 32)};
    if (lane < n) v = ld64(rws, voff, 0);
    const bool ok = v.x == (unsigned)tag && v.y == (unsigned)(tag >> 32);
    if (__all(ok)) break;
    __builtin_amdgcn_s_sleep(1);
    if (++spins > SPIN_CAP) { dead = true; if (lane == 0) __builtin_amdgcn_raw_buffer_store_b32(1u, rws, WS_ERR, 0, SC1); break; }
  }
}
template <class XL> DI void ensure(bf16x8& x, int s, XL xload, rsrc_t rws, bool& dead) {
  if (__builtin_expect(bad4(__builtin_bit_cast(u32x4, x)), 0)) {
    unsigned spins = 0;
    while (!dead) {
      __builtin_amdgcn_s_sleep(2);
      x = xload(s);
      if (!bad4(__builtin_bit_cast(u32x4, x))) break;
      if (++spins > SPIN_CAP) { dead = true; __builtin_amdgcn_raw_buffer_store_b32(1u, rws, WS_ERR, 0, SC1); }
    }
    __builtin_amdgcn_s_waitcnt(0x0F70);
  }
}
DI void set_flag(rsrc_t rws, unsigned off, u64 tag) { u32x2 v = {(unsigned)tag, (unsigned)(tag >> 32)}; st64(v, rws, off, 0); }

template <int NP> DI unsigned raw_voff(int j, int lane, unsigned rowpitch, unsigned cb0, unsigned cbs) {
  const int beta = 2 * j + (lane >> 5), oct = beta / NP, pair = beta % NP, r = (lane >> 2) & 7, q4 = lane & 3;
  return (unsigned)(8 * oct + r) * rowpitch + cb0 + (unsigned)pair * cbs + (unsigned)((((q4 >> 1) ^ (r >> 2) ^ oct) & 1) * 32 + (q4 & 1) * 16);
}
template <int NP, int ORD> DI bf16x8 raw_frag(const LAS char* kbase, int ct, int lane) {
  const int g = lane >> 4, q = (lane & 15) >> 2, pp = lane & 3, pair = ct >> 1, c = ct & 1;
  int o1, r1, o2, r2;
  if (ORD == 0) { o1 = g >> 1; r1 = 4 * (g & 1) + q; o2 = o1 + 2; r2 = r1; }
  else { o1 = 2 * (g >> 1); r1 = 4 * (g & 1) + q; o2 = o1 + 1; r2 = r1; }
  const s16x4 lo = trd(kbase + (o1 * NP + pair) * 512 + r1 * 64 + ((c ^ (r1 >> 2) ^ o1) & 1) * 32 + pp * 8);
  const s16x4 hi = trd(kbase + (o2 * NP + pair) * 512 + r2 * 64 + ((c ^ (r2 >> 2) ^ o2) & 1) * 32 + pp * 8);
  return __builtin_shufflevector(lo, hi, 0, 1, 2, 3, 4, 5, 6, 7);
}

template <int NT, int S0, int S1, int NX, int PD, bool VAL, class XL, class OFF, class HOOK>
DI void gemm_lds(f32x4 (&acc)[NT], bf16x8 (&X)[NX], const LAS char* wb, XL xload, OFF off, HOOK hook, rsrc_t rws, bool& dead) {
  bf16x8 wf[2][NT];
#pragma unroll
  for (int c = 0; c < NT; ++c) wf[0][c] = ldsfrag(wb + off(S0) + c * 1024);
  if (VAL && S0 == 0) ensure(X[0], 0, xload, rws, dead);
  SBAR();
#pragma unroll
  for (int s = S0; s < S1; ++s) {
    const int cur = (s - S0) & 1;
    if (PD > 0) {
#pragma unroll
      for (int e = 0; e < 2; ++e) { const int ix = PD + 2 * s + e; if (ix < NX) X[ix] = xload(ix); }
    }
    if (VAL && s + 1 < NX) ensure(X[s + 1], s + 1, xload, rws, dead);
    hook(s);
    __builtin_amdgcn_s_waitcnt(0xC07F);
    SBAR();
#pragma unroll
    for (int c = 0; c < NT; ++c) {
      if (s + 1 < S1) wf[cur ^ 1][c] = ldsfrag(wb + off(s + 1) + c * 1024);
      acc[c] = mfma16(wf[cur][c], X[s], acc[c]);
    }
#pragma unroll
    for (int c = 0; c < NT; ++c) {
      __builtin_amdgcn_sched_group_barrier(0x008, 1, 0);
      if (s + 1 < S1 && 2 * c < NT) __builtin_amdgcn_sched_group_barrier(0x100, 2, 0);
    }
    SBAR();
  }
}

#define STAMP(i) do {} while (0)
#define RT(i) do {} while (0)
#define STAMP_OUT() do {} while (0)

DI void stats_load(rsrc_t rws, unsigned base, int lane, u32x2 (&pv)[4]) {
  const int tok = lane & 15, g = lane >> 4;
#pragma unroll
  for (int i = 0; i < 4; ++i) pv[i] = ld64(rws, (unsigned)((4 * g + i) * 128 + tok * 8), base);
}
DI void stats_ensure(rsrc_t rws, unsigned base, int lane, u32x2 (&pv)[4], bool& dead) {
  if (__builtin_expect(__any(umax(umax(umax(pv[0].x, pv[0].y), umax(pv[1].x, pv[1].y)), umax(umax(pv[2].x, pv[2].y), umax(pv[3].x, pv[3].y))) == SENT) && !NOSYNC_, 0)) {
    unsigned spins = 0;
    while (!dead) {
      __builtin_amdgcn_s_sleep(2);
      stats_load(rws, base, lane, pv);
      if (!(bad2(pv[0]) || bad2(pv[1]) || bad2(pv[2]) || bad2(pv[3]))) break;
      if (++spins > SPIN_CAP) { dead = true; __builtin_amdgcn_raw_buffer_store_b32(1u, rws, WS_ERR, 0, SC1); }
    }
    __builtin_amdgcn_s_waitcnt(0x0F70);
  }
}
DI void stats_from_partials(const u32x2 (&pv)[4], float& mean, float& var) {
  float s = 0.f;
#pragma unroll
  for (int i = 0; i < 4; ++i) s += __uint_as_float(pv[i].x);
  s = quad_sum(s);
  mean = s * (1.0f / 512.0f);
  float m2 = 0.f;
#pragma unroll
  for (int i = 0; i < 4; ++i) { const float d = __uint_as_float(pv[i].x) * (1.0f / 32.0f) - mean; m2 += __uint_as_float(pv[i].y) + 32.0f * d * d; }
  m2 = quad_sum(m2);
  var = m2 * (1.0f / 512.0f);
}

DI void publish_x(rsrc_t rws, const float (&v)[8], unsigned xoff, unsigned stoff, int lane, u32x4& packed) {
  packed.x = cvtpk(v[0], v[1]); packed.y = cvtpk(v[2], v[3]); packed.z = cvtpk(v[4], v[5]); packed.w = cvtpk(v[6], v[7]);
  st128(packed, rws, (unsigned)lane * 16u, xoff);
  float r[8] = {bflo(packed.x), bfhi(packed.x), bflo(packed.y), bfhi(packed.y), bflo(packed.z), bfhi(packed.z), bflo(packed.w), bfhi(packed.w)};
  float s = 0.f;
#pragma unroll
  for (int i = 0; i < 8; ++i) s += r[i];
  s = quad_sum(s);
  const float mu = s * (1.0f / 32.0f);
  float m2 = 0.f;
#pragma unroll
  for (int i = 0; i < 8; ++i) { const float d = r[i] - mu; m2 += d * d; }
  m2 = quad_sum(m2);
  if (lane < 16) { u32x2 o = {__float_as_uint(s), __float_as_uint(m2)}; st64(o, rws, (unsigned)lane * 8u, stoff); }
}

template <bool FULL, class HOOK>
DI u32x4 attn_wave(const LAS char* lds, int hd, int qi, int jmin, int lane, HOOK hook) {
  const int ql = lane & 31, h = lane >> 5;
  const bf16x8 Q = ldsfrag(lds + A_Q + (hd * 2 + h) * 2048 + (32 * qi + ql) * 16);
  const LAS char* kb = lds + A_K + (hd * 2 + h) * PLN + (32 * qi + ql) * 16;
  const int G = lane >> 4, q4 = (lane & 15) >> 2, pp = lane & 3;
  const LAS char* vb = lds + ((G & 1) ? A_ONE : A_V + (hd * 2 + (pp >> 1)) * PLN) + (32 * qi + 4 * h + q4) * 16 + (pp & 1) * 8;
  constexpr float NEG = -1e30f;
  const f32x16 zero16 = {0.f,0.f,0.f,0.f,0.f,0.f,0.f,0.f,0.f,0.f,0.f,0.f,0.f,0.f,0.f,0.f};
  f32x16 biasD;
  const int dq = ql - 4 * h;
#pragma unroll
  for (int r = 0; r < 16; ++r) biasD[r] = ((r & 3) + 8 * (r >> 2) <= dq) ? 0.f : NEG;
  float m0 = NEG, m1 = NEG;
  {
    bf16x8 ka = ldsfrag(kb + 8 * 512), kn = ka;
    if (FULL || 7 >= jmin) kn = ldsfrag(kb + 7 * 512);
    f32x16 sa = mfma32(ka, Q, biasD), sb = sa;
    SBAR();
#pragma unroll
    for (int j = 8; j >= 0; --j) {
      f32x16& cur = (j & 1) ? sb : sa; f32x16& nxt = (j & 1) ? sa : sb;
      if (j >= 1 && (FULL || j - 1 >= jmin)) {
        const bf16x8 kj = kn;
        if (j >= 2 && (FULL || j - 2 >= jmin)) kn = ldsfrag(kb + (j - 2) * 512);
        if (j - 1 == 0) { f32x16 bf;
#pragma unroll
          for (int r = 0; r < 16; ++r) bf[r] = NEG - biasD[r];
          nxt = mfma32(kj, Q, bf); }
        else nxt = mfma32(kj, Q, zero16);
      }
      if (FULL || j >= jmin) {
#pragma unroll
        for (int r = 0; r < 16; r += 4) { m0 = max3f(m0, cur[r], cur[r + 1]); m1 = max3f(m1, cur[r + 2], cur[r + 3]); }
        asm volatile("" : "+v"(m0), "+v"(m1));
      }
      SBAR();
    }
  }
  const float m = swap32_max(fmaxf(m0, m1));
  f32x16 negm;
#pragma unroll
  for (int r = 0; r < 16; ++r) negm[r] = -m;
  f32x16 o = zero16;
  {
    bf16x8 ka = ldsfrag(kb + 8 * 512), kn = ka;
    if (FULL || 7 >= jmin) kn = ldsfrag(kb + 7 * 512);
    f32x16 sa = mfma32(ka, Q, biasD + negm), sb = sa;
    SBAR();
#pragma unroll
    for (int j = 8; j >= 0; --j) {
      f32x16& cur = (j & 1) ? sb : sa; f32x16& nxt = (j & 1) ? sa : sb;
      const bool have = FULL || j >= jmin;
      hook(j);
      s16x4 v0 = {}, v1 = {}, v2 = {}, v3 = {};
      if (have) { v0 = trd(vb + (32 * j) * 16); v1 = trd(vb + (32 * j + 8) * 16); v2 = trd(vb + (32 * j + 16) * 16); v3 = trd(vb + (32 * j + 24) * 16); }
      if (j >= 1 && (FULL || j - 1 >= jmin)) {
        const bf16x8 kj = kn;
        if (j >= 2 && (FULL || j - 2 >= jmin)) kn = ldsfrag(kb + (j - 2) * 512);
        if (j - 1 == 0) { f32x16 cf;
#pragma unroll
          for (int r = 0; r < 16; ++r) cf[r] = (NEG - biasD[r]) - m;
          nxt = mfma32(kj, Q, cf); }
        else nxt = mfma32(kj, Q, negm);
      }
      if (have) {
        unsigned pk[8];
#pragma unroll
        for (int r = 0; r < 8; ++r) pk[r] = cvtpk(__builtin_amdgcn_exp2f(cur[2 * r]), __builtin_amdgcn_exp2f(cur[2 * r + 1]));
        o = mfma32(__builtin_shufflevector(v0, v1, 0, 1, 2, 3, 4, 5, 6, 7), asfrag(u32x4{pk[0], pk[1], pk[2], pk[3]}), o);
        o = mfma32(__builtin_shufflevector(v2, v3, 0, 1, 2, 3, 4, 5, 6, 7), asfrag(u32x4{pk[4], pk[5], pk[6], pk[7]}), o);
      }
      SBAR();
    }
  }
  const float inv = __builtin_amdgcn_rcpf(o[8]);
  return u32x4{cvtpk(o[0] * inv, o[1] * inv), cvtpk(o[2] * inv, o[3] * inv), cvtpk(o[4] * inv, o[5] * inv), cvtpk(o[6] * inv, o[7] * inv)};
}

__global__ __launch_bounds__(512, 2) void mega(Params P) {
  __shared__ __attribute__((aligned(16))) char lds_[LDS_BYTES];
  const LAS char* lds = (const LAS char*)lds_;
  LAS char* ldsw = (LAS char*)lds_;
  const unsigned ldsb = (unsigned)(size_t)lds_;
  int w = rfl(threadIdx.x >> 6);
  int lane = (int)__builtin_amdgcn_mbcnt_hi(~0u, __builtin_amdgcn_mbcnt_lo(~0u, 0u));
  const int b = blockIdx.x;
  int t = 4 * ((b & 7) >> 1) + (b >> 6), p = 8 * (b & 1) + ((b >> 3) & 7);
  int hh = (p >> 2) & 1, cg = 4 * (p >> 3) + (p & 3);
  int tt = w & 3, kh = w >> 2;
  int tauA = 8 * t + w, tauB = 8 * t + 4 * hh + tt, ksB = 2 * cg + kh, u32i = 2 * t + hh;
  int tok = lane & 15, g = lane >> 4;
  const rsrc_t rws = __builtin_amdgcn_make_buffer_rsrc(P.ws, 0, (int)WS_END, 0x00020000);
  bool dead = false;
  STAMP(0); RT(59);
  if (w == 7) {
    u64 pc;
    asm volatile("s_getpc_b64 %0" : "=s"(pc));
    const rsrc_t rc = __builtin_amdgcn_make_buffer_rsrc((void*)pc, 0, CODE_BYTES, 0x00020000);
    touch8k(rc, (unsigned)lane * 128u, (unsigned)((b >> 3) % 5) * 8192u, ldsb + T_DUMP);
    touch8k(rws, (unsigned)lane << 20, 0u, ldsb + T_DUMP);
  }
  u64 tag = 0;
  u32x4 resid = {0u, 0u, 0u, 0u};
  auto nohook = [](int) INL {};
  auto noload = [](int) INL { return bf16x8{}; };

  int nL = 2;
  asm volatile("" : "+s"(nL));
#pragma unroll 1
  for (int L = 0; L < nL; ++L) {
    asm volatile("" : "+s"(L));
    const unsigned v16 = (unsigned)lane * 16u;
    auto load_wq = [&](int w0, int nw) INL {
      if (w >= w0) for (int sl = w - w0; sl < 16; sl += nw) { const unsigned src = WS_WQT + (unsigned)((p * 16 + (sl ^ (2 * (p & 7)))) * 6) * 1024u, dst = ldsb + MAIN + (unsigned)sl * 6144u;
        dma4k(rws, (unsigned)lane * 16u, src, dst); dma2k(rws, (unsigned)lane * 16u, src + 4096u, dst + 4096u); }
    };
    auto load_w1 = [&](int h0, int h1, int w0, int nw) INL {
      if (p < NP1 && w >= w0) for (int hs = h0 + w - w0; hs < h1; hs += nw) { const int sl = hs >> 1;
        dma4k(rws, (unsigned)lane * 16u, WS_W1T + (unsigned)((p * 16 + (sl ^ (2 * (p & 7)))) * 8 + (hs & 1) * 4) * 1024u, ldsb + L_W1 + (unsigned)hs * 4096u); }
    };
    auto w2_half = [&](int i, int e) INL {
      const int slot = i >= 19 ? i - 19 : i, k = i ^ (p & 3);
      dma4k(rws, (unsigned)lane * 16u, WS_W2T + (unsigned)((cg * 56 + e * 28 + k) * 4) * 1024u, ldsb + MAIN + (unsigned)(slot * 8 + e * 4) * 1024u);
    };
    auto load_w2 = [&](int i0, int i1, int w0, int nw) INL {
      if (w >= w0) for (int q = 2 * i0 + w - w0; q < 2 * i1; q += nw) w2_half(q >> 1, q & 1);
    };
    (void)v16;
    FRESH();
    {
      bf16x8 X[16];
      float mean = 0.f, var = 0.f;
      const int rotA = 2 * (p & 7);
      f32x4 akv[4];
#pragma unroll
      for (int c = 0; c < 4; ++c) akv[c] = f32x4{0.f, 0.f, 0.f, 0.f};
      if (L == 0) {
        const rsrc_t rx = __builtin_amdgcn_make_buffer_rsrc((void*)P.x, 0, S_ * D_ * 2, 0x00020000);
        if (w < 2) {
          const rsrc_t rq = __builtin_amdgcn_make_buffer_rsrc((void*)P.wqkv, 0, D_ * 3 * D_ * 2, 0x00020000);
#pragma unroll
          for (int jj = 0; jj < 3; ++jj) { const int j = 3 * w + jj; dma16(rq, raw_voff<3>(j, lane, 3072u, (unsigned)p * 64u, 1024u), rfl((unsigned)t * 32u * 3072u), rfl(ldsb + L_PRE + 18 * KB + (unsigned)j * 1024u)); }
        } else if (w < 4) {
          touch8k(rx, (unsigned)lane * 128u, rfl((unsigned)(128 * t + 16 * (p & 7)) * 1024u + (unsigned)(w - 2) * 8192u), ldsb + T_DUMP);
          if (p < NP1) {
            const rsrc_t r1 = __builtin_amdgcn_make_buffer_rsrc((void*)P.w1, 0, D_ * F_ * 2, 0x00020000);
#pragma unroll
            for (int jj = 0; jj < 4; ++jj) { const int j = 4 * (w - 2) + jj; dma16(r1, raw_voff<4>(j, lane, 3584u, (unsigned)p * 256u, 64u), rfl((unsigned)t * 32u * 3584u), rfl(ldsb + L_PRE + (unsigned)j * 1024u)); }
          }
        } else if (w == 4) {
          const rsrc_t ro = __builtin_amdgcn_make_buffer_rsrc((void*)P.wo, 0, D_ * D_ * 2, 0x00020000);
#pragma unroll
          for (int j = 0; j < 2; ++j) dma16(ro, raw_voff<1>(j, lane, 1024u, (unsigned)(cg * 128 + (u32i & 1) * 64), 0u), rfl((unsigned)(u32i >> 1) * 32u * 1024u), rfl(ldsb + L_PRE + 8 * KB + (unsigned)j * 1024u));
        } else if (w == 5 || (w == 6 && u32i < 24)) {
          const rsrc_t r2 = __builtin_amdgcn_make_buffer_rsrc((void*)P.w2, 0, F_ * D_ * 2, 0x00020000);
          const int ks = u32i + (w - 5) * 32;
#pragma unroll
          for (int j = 0; j < 4; ++j) dma16(r2, raw_voff<2>(j, lane, 1024u, (unsigned)cg * 128u, 64u), rfl((unsigned)ks * 32u * 1024u), rfl(ldsb + L_PRE + (10 + (w - 5) * 4) * KB + (unsigned)j * 1024u));
        }
        u32x2 epoch_raw = ld64(rws, WS_EPOCH, 0);
        STAMP(23);
        VMWAIT0();
        tag = ((((u64)rfl(epoch_raw.y)) << 32) | rfl(epoch_raw.x)) + 1ull;
        BAR();
        STAMP(1);
        if (w < 6) st128(__builtin_bit_cast(u32x4, raw_frag<3, 0>(lds + L_PRE + 18 * KB, w, lane)), rws, (unsigned)lane * 16u, rfl(WS_WQT + (unsigned)((p * 16 + t) * 6 + w) * 1024u));
        VMWAIT0();
        BAR();
        if (w == 0 && lane == 0) set_flag(rws, FL_WT + (unsigned)(t * 16 + p) * 128u, tag);
        STAMP(2);
        FRESH_V();
        if (p < NP1) st128(__builtin_bit_cast(u32x4, raw_frag<4, 0>(lds + L_PRE, w, lane)), rws, (unsigned)lane * 16u, rfl(WS_W1T + (unsigned)((p * 16 + t) * 8 + w) * 1024u));
        if (w >= 6) st128(__builtin_bit_cast(u32x4, raw_frag<1, 1>(lds + L_PRE + 8 * KB, w - 6, lane)), rws, (unsigned)lane * 16u, rfl(WS_WOT + (unsigned)((cg * 16 + (u32i >> 1)) * 4 + (u32i & 1) * 2 + (w - 6)) * 1024u));
        if (w < 4 || u32i < 24) { const int hs = w >> 2, ks = u32i + hs * 32;
          st128(__builtin_bit_cast(u32x4, raw_frag<2, 0>(lds + L_PRE + (10 + hs * 4) * KB, w & 3, lane)), rws, (unsigned)lane * 16u, rfl(WS_W2T + (unsigned)((cg * 56 + ks) * 4 + (w & 3)) * 1024u)); }
        u32x4 xr[16];
        const unsigned xo = (unsigned)((16 * tauA + tok) * 1024 + g * 16);
        if (w == 0) poll_flags(rws, FL_WT + (unsigned)((lane * 16 + p) * 128), 16, tag, lane, dead);
#pragma unroll
        for (int s = 0; s < 16; ++s) xr[s] = __builtin_amdgcn_raw_buffer_load_b128(rx, xo, (unsigned)((s ^ rotA) * 64), 0);
        BAR();
        STAMP(24);
        load_wq(0, 8);
        unsigned biasv = 0;
        if (w < 2) { if (p < NP1) biasv = P.b1[p * 128 + w * 64 + lane]; } else if (w == 2) biasv = P.b2[cg * 64 + lane];
        {
          const GAS u32x2* rp = (const GAS u32x2*)(P.x + (size_t)(16 * tauB + tok) * D_ + 32 * ksB + 4 * g);
          const u32x2 r0 = rp[0], r1 = rp[4];
          resid = u32x4{r0.x, r0.y, r1.x, r1.y};
        }
        FRESH_V();
        {
          float s = 0.f;
#pragma unroll
          for (int i = 0; i < 16; ++i) { const u32x4 v = xr[i];
            s += (bflo(v.x) + bfhi(v.x)) + (bflo(v.y) + bfhi(v.y)) + (bflo(v.z) + bfhi(v.z)) + (bflo(v.w) + bfhi(v.w)); asm volatile("" : "+v"(s)); SBAR(); }
          s = quad_sum(s); mean = s * (1.0f / 512.0f);
          float m2 = 0.f;
#pragma unroll
          for (int i = 0; i < 16; ++i) asm volatile("" : "+v"(xr[i]));
#pragma unroll
          for (int i = 0; i < 16; ++i) { const u32x4 v = xr[i];
#pragma unroll
            for (int e = 0; e < 4; ++e) { const float x0 = bflo(v[e]) - mean, x1 = bfhi(v[e]) - mean; m2 += x0 * x0 + x1 * x1; } asm volatile("" : "+v"(m2)); SBAR(); }
          m2 = quad_sum(m2); var = m2 * (1.0f / 512.0f);
#pragma unroll
          for (int i = 0; i < 16; ++i) {
            auto a0 = __builtin_amdgcn_permlane16_swap(xr[i].x, xr[i].z, false, false), a1 = __builtin_amdgcn_permlane16_swap(xr[i].y, xr[i].w, false, false);
            auto b0 = __builtin_amdgcn_permlane32_swap(a0[0], a0[1], false, false), b1 = __builtin_amdgcn_permlane32_swap(a1[0], a1[1], false, false);
            X[i] = asfrag(u32x4{b0[0], b1[0], b0[1], b1[1]});
            SBAR();
          }
        }
        if (w < 3) *(LAS float*)(ldsw + (w < 2 ? T_B1 + (w * 64 + lane) * 4 : T_B2 + lane * 4)) = bf2f(biasv);
        VMWAIT0();
        BAR();
        STAMP(27);
        if (w < 6) {
          f32x4 cs = {0.f, 0.f, 0.f, 0.f};
#pragma unroll
          for (int s = 0; s < 16; ++s) cs = mfma16(ldsfrag(lds + MAIN + (s * 6 + w) * KB + lane * 16), ones_frag(), cs);
          if (tok == 0) *(LAS f32x4*)(ldsw + T_CSQ + (w * 16 + 4 * g) * 4) = cs;
        }
        STAMP(16 + 32 * L);
        gemm_lds<4, 0, 16, 16, 0, false>(akv, X, lds + MAIN + 2 * KB + lane * 16, noload, [](int s) INL { return s * 6 * KB; }, nohook, rws, dead);
        BAR();
      } else {
        STAMP(16 + 32 * L);
        u32x2 pv[4];
        stats_load(rws, WS_ST2 + (unsigned)tauA * 2048u, lane, pv);
        const unsigned xb = WS_X2 + (unsigned)(tauA * 16 + rotA) * 1024u;
        auto xl = [&](int s) INL { return asfrag(ld128(rws, (unsigned)lane * 16u, xb ^ ((unsigned)s << 10))); };
#pragma unroll
        for (int s = 0; s < 8; ++s) X[s] = xl(s);
        stats_from_partials(pv, mean, var);
        gemm_lds<4, 0, 16, 16, 8, false>(akv, X, lds + MAIN + 2 * KB + lane * 16, xl, [](int s) INL { return s * 6 * KB; }, nohook, rws, dead);
      }
      FRESH();
      const float rs = __builtin_amdgcn_rsqf(var + EPS), rm = rs * mean;
      auto qkv_pack = [&](const f32x4& a0, const f32x4& a1, int c0, float sc) INL {
        const f32x4 cs0 = *(const LAS f32x4*)(lds + T_CSQ + (c0 * 16 + 4 * g) * 4), cs1 = *(const LAS f32x4*)(lds + T_CSQ + (c0 * 16 + 16 + 4 * g) * 4);
        float v[8];
#pragma unroll
        for (int j = 0; j < 4; ++j) { v[j] = (rs * a0[j] - rm * cs0[j]) * sc; v[4 + j] = (rs * a1[j] - rm * cs1[j]) * sc; }
        auto s0 = __builtin_amdgcn_permlane16_swap(cvtpk(v[0], v[1]), cvtpk(v[4], v[5]), false, false);
        auto s1 = __builtin_amdgcn_permlane16_swap(cvtpk(v[2], v[3]), cvtpk(v[6], v[7]), false, false);
        return u32x4{s0[0], s1[0], s0[1], s1[1]};
      };
      {
        const u32x4 ko = qkv_pack(akv[0], akv[1], 2, 1.0f), vo = qkv_pack(akv[2], akv[3], 4, 1.0f);
        st128(ko, rws, (unsigned)(PLANE * 2048 + TL * 16), WS_KG + (unsigned)((L * 16 + t) * 16 + p) * 8192u);
        st128(vo, rws, (unsigned)(PLANE * 2048 + TL * 16), WS_VG + (unsigned)((L * 16 + t) * 16 + p) * 8192u);
        *(LAS u32x4*)(ldsw + A_K + PLANE * PLN + (256 + TL) * 16) = ko;
        *(LAS u32x4*)(ldsw + A_V + PLANE * PLN + (256 + TL) * 16) = vo;
      }
      STAMP(3 + 32 * L);
      FRESH();
      f32x4 aq[2] = {f32x4{0.f, 0.f, 0.f, 0.f}, f32x4{0.f, 0.f, 0.f, 0.f}};
      gemm_lds<2, 0, 8, 16, 0, false>(aq, X, lds + MAIN + lane * 16, noload, [](int s) INL { return s * 6 * KB; }, nohook, rws, dead);
      VMWAIT0();
      BAR();
      if (w == 0 && lane == 0) set_flag(rws, flag_row(0, K_KV) + (unsigned)(L * 5 * 256 + t * 16 + p) * 128u, tag);
      gemm_lds<2, 8, 16, 16, 0, false>(aq, X, lds + MAIN + lane * 16, noload, [](int s) INL { return s * 6 * KB; }, nohook, rws, dead);
      STAMP(4 + 32 * L);
      FRESH();
      const u32x4 qo = qkv_pack(aq[0], aq[1], 0, QSCALE);
      if (w == 0) {
        if (L == 0) {
          const int grp = lane >> 4, pg = grp == 0 ? p : 8 * (cg >> 2) + 4 * (grp - 1) + (cg & 3);
          poll_flags(rws, flag_row(0, K_KV) + (unsigned)(((lane & 15) * 16 + pg) * 128), 48, tag, lane, dead);
        } else poll_flags(rws, flag_row(1, K_KV) + (unsigned)(((t - 1 - lane) * 16 + p) * 128), t < 2 ? t : 2, tag, lane, dead);
      }
      BAR();
      STAMP(5 + 32 * L);
      FRESH();
      {
        const int d = 1 + (w >> 2);
        u32x4 hv[4] = {u32x4{}, u32x4{}, u32x4{}, u32x4{}};
        if (t - d >= 0) {
          const unsigned hb = ((w & 2) ? WS_VG : WS_KG) + (unsigned)((L * 16 + t - d) * 16 + p) * 8192u + (unsigned)(w & 1) * 4096u;
#pragma unroll
          for (int i = 0; i < 4; ++i) hv[i] = ld128(rws, (unsigned)lane * 16u, hb + (unsigned)i * 1024u);
        }
        *(LAS u32x4*)(ldsw + A_Q + PLANE * 2048 + TL * 16) = qo;
        if (w * 64 + lane < 400) *(LAS u32x4*)(ldsw + A_ONE + (w * 64 + lane) * 16) = __builtin_bit_cast(u32x4, ones_frag());
        if (t - d >= 0) {
#pragma unroll
          for (int i = 0; i < 4; ++i) *(LAS u32x4*)(ldsw + ((w & 2) ? A_V : A_K) + (2 * (w & 1) + (i >> 1)) * PLN + ((2 - d) * 128 + (i & 1) * 64 + lane) * 16) = hv[i];
        }
      }
      BAR();
      STAMP(6 + 32 * L);
    }
    FRESH();
    {
      if (w >= 4 && p < NP1) touch8k(rws, (unsigned)lane * 128u, WS_W1T + (unsigned)(p * 128 + (t & 3) * 32 + (w - 4) * 8) * 1024u, ldsb + T_DUMP);
      STAMP(17 + 32 * L);
      u32x4 ov;
      if (t >= 2) ov = attn_wave<true>(lds, w & 1, w >> 1, 0, lane, nohook); else ov = attn_wave<false>(lds, w & 1, w >> 1, 8 - 4 * t - (w >> 1), lane, nohook);
      FRESH();
      st128(ov, rws, (unsigned)(((lane & 31) >> 4) * 16384 + ((2 * (w & 1) + (lane >> 5)) * 16 + (lane & 15)) * 16), WS_OG + (unsigned)((L * 128 + 8 * t + 2 * (w >> 1)) * 16 + p) * 1024u);
      STAMP(18 + 32 * L);
      if (w < 4) {
#pragma unroll
        for (int i = 0; i < 4; ++i) { const int sl = w + 4 * i; dma4k(rws, (unsigned)lane * 16u, WS_WOT + (unsigned)((cg * 16 + (sl ^ (2 * (p & 3)))) * 4) * 1024u, ldsb + L_WO + (unsigned)sl * 4096u); }
      }
      VMWAIT0();
      BAR();
      if (w == 0 && lane == 0) set_flag(rws, flag_row(0, K_O) + (unsigned)(L * 5 * 256 + t * 16 + p) * 128u, tag);
      STAMP(7 + 32 * L);
      load_w1(14, 32, 1, 7);
      if (w == 0) poll_flags(rws, flag_row(0, K_O) + (unsigned)(L * 5 * 256 + t * 16 + lane) * 128u, 16, tag, lane, dead);
      BAR();
      STAMP(8 + 32 * L);
    }
    FRESH();
    {
      bf16x8 Xo[8];
      const unsigned xb = WS_OG + (unsigned)((L * 128 + tauB) * 16 + 8 * kh + 2 * (p & 3)) * 1024u;
      auto xl = [&](int i) INL { return asfrag(ld128(rws, (unsigned)lane * 16u, xb ^ ((unsigned)i << 10))); };
#pragma unroll
      for (int i = 0; i < 8; ++i) Xo[i] = xl(i);
      f32x4 acc[4];
#pragma unroll
      for (int c = 0; c < 4; ++c) acc[c] = f32x4{0.f, 0.f, 0.f, 0.f};
      gemm_lds<4, 0, 8, 8, 0, false>(acc, Xo, lds + L_WO + kh * 32 * KB + lane * 16, xl, [](int s) INL { return s * 4 * KB; }, nohook, rws, dead);
      STAMP(19 + 32 * L);
      FRESH();
      *(LAS f32x4*)(ldsw + MAIN + (w * 2 + 0) * KB + lane * 16) = kh ? acc[0] : acc[2];
      *(LAS f32x4*)(ldsw + MAIN + (w * 2 + 1) * KB + lane * 16) = kh ? acc[1] : acc[3];
      BAR();
      const f32x4 o0 = *(const LAS f32x4*)(lds + MAIN + ((w ^ 4) * 2 + 0) * KB + lane * 16), o1 = *(const LAS f32x4*)(lds + MAIN + ((w ^ 4) * 2 + 1) * KB + lane * 16);
      const f32x4 k0 = kh ? acc[2] : acc[0], k1 = kh ? acc[3] : acc[1];
      float v[8];
      v[0] = k0[0] + o0[0] + bflo(resid.x); v[1] = k0[1] + o0[1] + bfhi(resid.x); v[2] = k0[2] + o0[2] + bflo(resid.y); v[3] = k0[3] + o0[3] + bfhi(resid.y);
      v[4] = k1[0] + o1[0] + bflo(resid.z); v[5] = k1[1] + o1[1] + bfhi(resid.z); v[6] = k1[2] + o1[2] + bflo(resid.w); v[7] = k1[3] + o1[3] + bfhi(resid.w);
      publish_x(rws, v, WS_X1 + (unsigned)((L * 128 + tauB) * 16 + ksB) * 1024u, WS_ST1 + (unsigned)((L * 128 + tauB) * 16 + ksB) * 128u, lane, resid);
      VMWAIT0();
      BAR();
      if (w == 0 && lane == 0) set_flag(rws, flag_row(0, K_X1) + (unsigned)(L * 5 * 256 + t * 16 + p) * 128u, tag);
      STAMP(9 + 32 * L);
      load_w1(0, 14, 1, 7);
      if (w >= 4) touch8k(rws, (unsigned)lane * 128u, WS_W2T + (unsigned)(cg * 224 + ((t & 3) * 2 + hh) * 28 + (w - 4) * 8) * 1024u, ldsb + T_DUMP);
      if (w == 0) poll_flags(rws, flag_row(0, K_X1) + (unsigned)(L * 5 * 256 + t * 16 + lane) * 128u, 16, tag, lane, dead);
      if (w >= 4) asm volatile("s_waitcnt vmcnt(1)" ::: "memory"); else VMWAIT0();
      BAR();
      STAMP(10 + 32 * L);
    }
    FRESH();
    if (p < NP1) {
      bf16x8 X1[16];
      u32x2 pv1[4];
      stats_load(rws, WS_ST1 + (unsigned)(L * 128 + tauA) * 2048u, lane, pv1);
      const unsigned xb = WS_X1 + (unsigned)((L * 128 + tauA) * 16 + 2 * (p & 7)) * 1024u;
      auto xl = [&](int s) INL { return asfrag(ld128(rws, (unsigned)lane * 16u, xb ^ ((unsigned)s << 10))); };
#pragma unroll
      for (int s = 0; s < 8; ++s) X1[s] = xl(s);
      if (L == 0) {
        f32x4 cs = {0.f, 0.f, 0.f, 0.f};
#pragma unroll
        for (int s = 0; s < 16; ++s) cs = mfma16(ldsfrag(lds + L_W1 + (s * 8 + w) * KB + lane * 16), ones_frag(), cs);
        if (tok == 0) *(LAS f32x4*)(ldsw + T_CS1 + (w * 16 + 4 * g) * 4) = cs;
      }
      float mean1, var1;
      stats_from_partials(pv1, mean1, var1);
      STAMP(26 + 32 * L);
      f32x4 acc[8];
#pragma unroll
      for (int c = 0; c < 8; ++c) acc[c] = f32x4{0.f, 0.f, 0.f, 0.f};
      auto hook = [&](int s) INL {
        if (s == 13 && w < 6) w2_half(w >> 1, w & 1);
        if (s == 14 && L == 0 && w < 3) touch8k(rws, (unsigned)lane * 128u, WS_WQT + (unsigned)(p * 96 + (t & 3) * 24 + w * 8) * 1024u, ldsb + T_DUMP);
      };
      gemm_lds<8, 0, 16, 16, 8, false>(acc, X1, lds + L_W1 + lane * 16, xl, [](int s) INL { return s * 8 * KB; }, hook, rws, dead);
      BAR();
      STAMP(11 + 32 * L);
      FRESH_V();
      const float r1 = __builtin_amdgcn_rsqf(var1 + EPS), r2 = __builtin_amdgcn_rsqf(var1 * r1 * r1 + EPS), rs = r1 * r2, rm = rs * mean1;
      constexpr float C1 = -2.0f * 0.7978845608028654f * 1.4426950408889634f, C2 = C1 * 0.044715f;
      const f32x2 rs2 = {rs, rs}, nrm2 = {-rm, -rm}, c1v = {C1, C1}, c2v = {C2, C2}, one2 = {1.0f, 1.0f};
#pragma unroll
      for (int u = 0; u < 4; ++u) {
        f32x2 av[4], e[4];
#pragma unroll
        for (int hf = 0; hf < 2; ++hf) {
          const int c = 2 * u + hf;
          const f32x4 cs = *(const LAS f32x4*)(lds + T_CS1 + (c * 16 + 4 * g) * 4), bb = *(const LAS f32x4*)(lds + T_B1 + (c * 16 + 4 * g) * 4);
          av[2 * hf] = rs2 * f32x2{acc[c][0], acc[c][1]} + (nrm2 * f32x2{cs[0], cs[1]} + f32x2{bb[0], bb[1]});
          av[2 * hf + 1] = rs2 * f32x2{acc[c][2], acc[c][3]} + (nrm2 * f32x2{cs[2], cs[3]} + f32x2{bb[2], bb[3]});
        }
#pragma unroll
        for (int i = 0; i < 4; ++i) e[i] = av[i] * (c2v * (av[i] * av[i]) + c1v);
#pragma unroll
        for (int i = 0; i < 4; ++i) e[i] = f32x2{__builtin_amdgcn_exp2f(e[i].x), __builtin_amdgcn_exp2f(e[i].y)} + one2;
#pragma unroll
        for (int i = 0; i < 4; ++i) e[i] = av[i] * f32x2{__builtin_amdgcn_rcpf(e[i].x), __builtin_amdgcn_rcpf(e[i].y)};
        st128(u32x4{cvtpk2(e[0]), cvtpk2(e[1]), cvtpk2(e[2]), cvtpk2(e[3])}, rws, (unsigned)lane * 16u, WS_HG + (unsigned)((L * 128 + tauA) * 56 + 4 * p + u) * 1024u);
        SBAR();
        w2_half(3 + 4 * u + (w >> 1), w & 1);
        SBAR();
      }
      STAMP(20 + 32 * L);
      asm volatile("s_waitcnt vmcnt(4)" ::: "memory");
      BAR();
      if (w == 0 && lane == 0) set_flag(rws, flag_row(0, K_H) + (unsigned)(L * 5 * 256 + t * 16 + p) * 128u, tag);
    } else {
      if (L == 1 && b == 255 && w == 0) {
        bool d2 = dead;
#pragma unroll 1
        for (int i = 0; i < 4; ++i) poll_flags(rws, FL_WT + (unsigned)(i * 64 + lane) * 128u, 64, tag, lane, d2);
        if (lane == 0) set_flag(rws, WS_EPOCH, tag);
      }
      load_w2(0, 19, 0, 8);
    }
    FRESH();
    {
      STAMP(12 + 32 * L);
      if (w == 0) poll_flags(rws, flag_row(0, K_H) + (unsigned)(L * 5 * 256 + t * 16 + lane) * 128u, NP1, tag, lane, dead);
      VMWAIT0();
      BAR();
      STAMP(13 + 32 * L);
      bf16x8 Xh[28];
      const unsigned xb = WS_HG + (unsigned)((L * 128 + tauB) * 56 + 28 * kh + (p & 3)) * 1024u;
      auto xl = [&](int i) INL { return asfrag(ld128(rws, (unsigned)lane * 16u, (xb ^ ((unsigned)(i & 3) << 10)) + (unsigned)(i & ~3) * 1024u)); };
#pragma unroll
      for (int i = 0; i < 10; ++i) Xh[i] = xl(i);
      f32x4 acc[4];
#pragma unroll
      for (int c = 0; c < 4; ++c) acc[c] = f32x4{0.f, 0.f, 0.f, 0.f};
      gemm_lds<4, 0, 9, 28, 10, false>(acc, Xh, lds + MAIN + kh * 4 * KB + lane * 16, xl, [](int s) INL { return s * 8 * KB; }, nohook, rws, dead);
      STAMP(21 + 32 * L);
      BAR();
      load_w2(19, 28, 0, 8);
      gemm_lds<4, 9, 19, 28, 10, false>(acc, Xh, lds + MAIN + kh * 4 * KB + lane * 16, xl, [](int s) INL { return s * 8 * KB; }, nohook, rws, dead);
      VMWAIT0();
      BAR();
      STAMP(22 + 32 * L);
      gemm_lds<4, 19, 28, 28, 10, false>(acc, Xh, lds + MAIN + kh * 4 * KB + lane * 16, xl, [](int s) INL { return (s - 19) * 8 * KB; }, nohook, rws, dead);
      STAMP(14 + 32 * L);
      FRESH();
      *(LAS f32x4*)(ldsw + L_RED2 + (w * 2 + 0) * KB + lane * 16) = kh ? acc[0] : acc[2];
      *(LAS f32x4*)(ldsw + L_RED2 + (w * 2 + 1) * KB + lane * 16) = kh ? acc[1] : acc[3];
      BAR();
      const f32x4 o0 = *(const LAS f32x4*)(lds + L_RED2 + ((w ^ 4) * 2 + 0) * KB + lane * 16), o1 = *(const LAS f32x4*)(lds + L_RED2 + ((w ^ 4) * 2 + 1) * KB + lane * 16);
      const f32x4 k0 = kh ? acc[2] : acc[0], k1 = kh ? acc[3] : acc[1];
      const f32x4 b0 = *(const LAS f32x4*)(lds + T_B2 + (kh * 32 + 4 * g) * 4), b1v = *(const LAS f32x4*)(lds + T_B2 + (kh * 32 + 16 + 4 * g) * 4);
      float v[8];
      v[0] = k0[0] + o0[0] + b0[0] + bflo(resid.x); v[1] = k0[1] + o0[1] + b0[1] + bfhi(resid.x); v[2] = k0[2] + o0[2] + b0[2] + bflo(resid.y); v[3] = k0[3] + o0[3] + b0[3] + bfhi(resid.y);
      v[4] = k1[0] + o1[0] + b1v[0] + bflo(resid.z); v[5] = k1[1] + o1[1] + b1v[1] + bfhi(resid.z); v[6] = k1[2] + o1[2] + b1v[2] + bflo(resid.w); v[7] = k1[3] + o1[3] + b1v[3] + bfhi(resid.w);
      if (L == 1) {
        const unsigned a0 = cvtpk(v[0], v[1]), a1 = cvtpk(v[2], v[3]), c0 = cvtpk(v[4], v[5]), c1 = cvtpk(v[6], v[7]);
        auto s0 = __builtin_amdgcn_permlane16_swap(a0, c0, false, false);
        auto s1 = __builtin_amdgcn_permlane16_swap(a1, c1, false, false);
        const u32x4 o = {s0[0], s1[0], s0[1], s1[1]};
        const int choff = (g & 1) * 16 + (g >> 1) * 8;
        *(GAS u32x4*)(P.out + (size_t)(16 * tauB + tok) * D_ + 32 * ksB + choff) = o;
        STAMP(15 + 32 * L); RT(60); STAMP_OUT();
        break;
      }
      publish_x(rws, v, WS_X2 + (unsigned)(tauB * 16 + ksB) * 1024u, WS_ST2 + (unsigned)(tauB * 16 + ksB) * 128u, lane, resid);
      VMWAIT0();
      BAR();
      if (w == 0 && lane == 0) set_flag(rws, flag_row(0, K_X2) + (unsigned)(t * 16 + p) * 128u, tag);
      load_wq(1, 7);
      if (w == 0) poll_flags(rws, flag_row(0, K_X2) + (unsigned)(t * 16 + lane) * 128u, 16, tag, lane, dead);
      VMWAIT0();
      BAR();
      STAMP(15 + 32 * L);
    }
  }
}

extern "C" void kernel_launch(void* const* d_in, const int* in_sizes, int n_in, void* d_out, int out_size, void* d_ws, size_t ws_size, hipStream_t stream) {
  Params P;
  P.x = (const u16*)d_in[0]; P.wqkv = (const u16*)d_in[1]; P.wo = (const u16*)d_in[2];
  P.w1 = (const u16*)d_in[4]; P.b1 = (const u16*)d_in[5]; P.w2 = (const u16*)d_in[6]; P.b2 = (const u16*)d_in[7];
  P.out = (u16*)d_out; P.ws = (unsigned char*)d_ws;
  mega<<<256, 512, 0, stream>>>(P);
}
```

```cpp
#include <hip/hip_runtime.h>
#include <stdint.h>

typedef unsigned short u16;
typedef unsigned long long u64;
typedef short bf16x8 __attribute__((ext_vector_type(8)));
typedef short s16x4 __attribute__((ext_vector_type(4)));
typedef float f32x2 __attribute__((ext_vector_type(2)));
typedef float f32x4 __attribute__((ext_vector_type(4)));
typedef float f32x16 __attribute__((ext_vector_type(16)));
typedef unsigned u32x4 __attribute__((ext_vector_type(4)));
typedef unsigned u32x2 __attribute__((ext_vector_type(2)));
typedef __bf16 bf16x2_t __attribute__((ext_vector_type(2)));
#define LAS __attribute__((address_space(3)))
#define GAS __attribute__((address_space(1)))
#define DI __device__ __forceinline__
#define INL __attribute__((always_inline))
typedef __amdgpu_buffer_rsrc_t rsrc_t;

constexpr int S_ = 2048, D_ = 512, F_ = 1792;
constexpr float EPS = 1e-5f;
constexpr float QSCALE = 0.25f * 1.4426950408889634f;
constexpr int NP1 = 14;

constexpr unsigned WS_EPOCH = 0, WS_ERR = 128, WS_FLAGS = 4096;
enum { K_KV = 0, K_O = 1, K_X1 = 2, K_H = 3, K_X2 = 4 };
constexpr unsigned flag_row(int L, int kind) { return WS_FLAGS + (unsigned)((L * 5 + kind) * 256 * 128); }
constexpr unsigned FL_WT = flag_row(1, K_X2);
constexpr unsigned WS_WQT = 0x60000;
constexpr unsigned WS_WOT = WS_WQT + 16 * 96 * 1024;
constexpr unsigned WS_W1T = WS_WOT + 8 * 64 * 1024;
constexpr unsigned WS_W2T = WS_W1T + NP1 * 128 * 1024;
constexpr unsigned WS_KG = WS_W2T + 8 * 224 * 1024;
constexpr unsigned WS_VG = WS_KG + 2 * 256 * 8192;
constexpr unsigned WS_OG = WS_VG + 2 * 256 * 8192;
constexpr unsigned WS_X1 = WS_OG + 2 * 128 * 16 * 1024;
constexpr unsigned WS_ST1 = WS_X1 + 2 * 128 * 16 * 1024;
constexpr unsigned WS_HG = WS_ST1 + 2 * 128 * 16 * 128;
constexpr unsigned WS_X2 = WS_HG + 2 * 128 * 56 * 1024;
constexpr unsigned WS_ST2 = WS_X2 + 128 * 16 * 1024;
constexpr unsigned WS_DBG = WS_ST2 + 128 * 16 * 128;
constexpr unsigned WS_END = WS_DBG + 256 * 8 * 128 * 8;

constexpr int KB = 1024;
constexpr int CODE_BYTES = 36 * 1024;
constexpr int LDS_BYTES = 163840;
constexpr int T_CSQ = 0;
constexpr int T_CS1 = 512;
constexpr int T_B1 = 1024;
constexpr int T_B2 = 1536;
constexpr int T_DUMP = 1792;
constexpr int MAIN = 2048;
constexpr int PLN = 384 * 16 + 64;
constexpr int A_K = MAIN + 96 * KB, A_V = A_K + 4 * PLN, A_Q = MAIN, A_ONE = A_Q + 4 * 2048;
static_assert(A_V + 4 * PLN <= LDS_BYTES && A_ONE + 6400 <= MAIN + 16 * KB, "attention buffers");
constexpr int L_W1 = MAIN + 30 * KB;
constexpr int L_WO = MAIN + 16 * KB;
constexpr int L_PRE = MAIN + 132 * KB;
constexpr int XROW = 1040;
static_assert(128 * XROW <= 132 * KB && L_PRE + 24 * KB <= LDS_BYTES, "prologue scratch");
constexpr int L_RED2 = MAIN + 96 * KB;
static_assert(L_W1 + 128 * KB <= LDS_BYTES && L_WO + 64 * KB <= LDS_BYTES, "images");

template <int V> struct IC { static constexpr int value = V; };
struct Params {
  const u16 *x, *wqkv, *wo, *w1, *b1, *w2, *b2;
  u16* out;
  unsigned char* ws;
};

DI float bf2f(unsigned v) { return __uint_as_float(v << 16); }
DI float bflo(unsigned v) { return __uint_as_float(v << 16); }
DI float bfhi(unsigned v) { return __uint_as_float(v & 0xffff0000u); }
DI unsigned cvtpk(float lo, float hi) { f32x2 v = {lo, hi}; bf16x2_t b = __builtin_convertvector(v, bf16x2_t); return __builtin_bit_cast(unsigned, b); }
DI unsigned cvtpk2(f32x2 v) { bf16x2_t b = __builtin_convertvector(v, bf16x2_t); return __builtin_bit_cast(unsigned, b); }
DI f32x4 mfma16(bf16x8 a, bf16x8 b, f32x4 c) { return __builtin_amdgcn_mfma_f32_16x16x32_bf16(a, b, c, 0, 0, 0); }
DI f32x16 mfma32(bf16x8 a, bf16x8 b, f32x16 c) { return __builtin_amdgcn_mfma_f32_32x32x16_bf16(a, b, c, 0, 0, 0); }
DI bf16x8 ones_frag() { unsigned o = 0x3F803F80u; asm volatile("" : "+v"(o)); return __builtin_bit_cast(bf16x8, u32x4{o, o, o, o}); }
DI bf16x8 asfrag(u32x4 v) { return __builtin_bit_cast(bf16x8, v); }
DI s16x4 trd(const LAS char* p) { return __builtin_bit_cast(s16x4, __builtin_amdgcn_ds_read_tr16_b64_v4i16((LAS s16x4*)p)); }
DI bf16x8 ldsfrag(const LAS char* p) { return *(const LAS bf16x8*)p; }
DI float max3f(float a, float b, float c) { return __builtin_fmaxf(__builtin_fmaxf(a, b), c); }
DI float swap32_add(float x) { auto r = __builtin_amdgcn_permlane32_swap(__float_as_uint(x), __float_as_uint(x), false, false); return __uint_as_float(r[0]) + __uint_as_float(r[1]); }
DI float swap16_add(float x) { auto r = __builtin_amdgcn_permlane16_swap(__float_as_uint(x), __float_as_uint(x), false, false); return __uint_as_float(r[0]) + __uint_as_float(r[1]); }
DI float quad_sum(float x) { return swap32_add(swap16_add(x)); }
DI float swap32_max(float x) { auto r = __builtin_amdgcn_permlane32_swap(__float_as_uint(x), __float_as_uint(x), false, false); return fmaxf(__uint_as_float(r[0]), __uint_as_float(r[1])); }

#define FRESH() do { asm volatile("" : "+v"(lane), "+s"(w), "+s"(t), "+s"(p)); tok = lane & 15; g = lane >> 4; hh = (p >> 2) & 1; cg = 4 * (p >> 3) + (p & 3); tt = w & 3; kh = w >> 2; \
  tauA = 8 * t + w; tauB = 8 * t + 4 * hh + tt; ksB = 2 * cg + kh; u32i = 2 * t + hh; } while (0)
#define PLANE ((g & 1) * 2 + (g >> 1))
#define TL (16 * w + tok)
#define FRESH_V() do { asm volatile("" : "+v"(lane)); tok = lane & 15; g = lane >> 4; } while (0)
#define BAR() asm volatile("s_waitcnt lgkmcnt(0)\n\ts_barrier" ::: "memory")
#define VMWAIT0() asm volatile("s_waitcnt vmcnt(0)" ::: "memory")
#define SBAR() __builtin_amdgcn_sched_barrier(0)
constexpr int SC1 = 16;
#define XAUX 16
DI u32x4 ld128(rsrc_t r, unsigned voff, unsigned soff) { return __builtin_amdgcn_raw_buffer_load_b128(r, voff, soff, XAUX); }
DI u32x2 ld64(rsrc_t r, unsigned voff, unsigned soff) { return __builtin_amdgcn_raw_buffer_load_b64(r, voff, soff, SC1); }
DI void st128(u32x4 v, rsrc_t r, unsigned voff, unsigned soff) { __builtin_amdgcn_raw_buffer_store_b128(v, r, voff + soff, 0, SC1); }
DI void st64(u32x2 v, rsrc_t r, unsigned voff, unsigned soff) { __builtin_amdgcn_raw_buffer_store_b64(v, r, voff + soff, 0, SC1); }
DI unsigned rfl(unsigned x) { return __builtin_amdgcn_readfirstlane(x); }
DI void dma16(rsrc_t r, unsigned voff, unsigned soff, unsigned ldsaddr) {
  asm volatile("s_mov_b32 m0, %0\n\ts_nop 3\n\tbuffer_load_dwordx4 %1, %2, %3 offen lds" :: "s"(rfl(ldsaddr)), "v"(voff), "s"(r), "s"(rfl(soff)) : "memory");
}
DI void dma16_sc1(rsrc_t r, unsigned voff, unsigned soff, unsigned ldsaddr) {
  asm volatile("s_mov_b32 m0, %0\n\ts_nop 3\n\tbuffer_load_dwordx4 %1, %2, %3 offen sc1 lds" :: "s"(rfl(ldsaddr)), "v"(voff), "s"(r), "s"(rfl(soff)) : "memory");
}
DI void dma4k(rsrc_t r, unsigned voff, unsigned soff, unsigned ldsaddr) {
  asm volatile("s_mov_b32 m0, %0\n\ts_nop 3\n\tbuffer_load_dwordx4 %1, %2, %3 offen sc1 lds\n\tbuffer_load_dwordx4 %1, %2, %3 offen offset:1024 sc1 lds\n\t"
               "buffer_load_dwordx4 %1, %2, %3 offen offset:2048 sc1 lds\n\tbuffer_load_dwordx4 %1, %2, %3 offen offset:3072 sc1 lds" :: "s"(rfl(ldsaddr)), "v"(voff), "s"(r), "s"(rfl(soff)) : "memory");
}
DI void dma2k(rsrc_t r, unsigned voff, unsigned soff, unsigned ldsaddr) {
  asm volatile("s_mov_b32 m0, %0\n\ts_nop 3\n\tbuffer_load_dwordx4 %1, %2, %3 offen sc1 lds\n\tbuffer_load_dwordx4 %1, %2, %3 offen offset:1024 sc1 lds" :: "s"(rfl(ldsaddr)), "v"(voff), "s"(r), "s"(rfl(soff)) : "memory");
}
DI void touch8k(rsrc_t r, unsigned voff, unsigned soff, unsigned ldsdump) {
  asm volatile("s_mov_b32 m0, %0\n\ts_nop 3\n\tbuffer_load_dword %1, %2, %3 offen sc1 lds" :: "s"(rfl(ldsdump)), "v"(voff), "s"(r), "s"(rfl(soff)) : "memory");
}

constexpr unsigned SENT = 0xFFFFFFFFu;
constexpr bool NOSYNC_ = false;
DI unsigned san(unsigned v) { return v < SENT - 1u ? v : SENT - 1u; }
DI u32x4 san4(u32x4 v) { return u32x4{san(v.x), san(v.y), san(v.z), san(v.w)}; }
DI unsigned umax(unsigned a, unsigned b) { return a > b ? a : b; }
DI bool bad4(u32x4 v) {
  return __any(umax(umax(v.x, v.y), umax(v.z, v.w)) == SENT); }
DI bool bad2(u32x2 v) {
  return __any(umax(v.x, v.y) == SENT); }
constexpr unsigned SPIN_CAP = 1u << 14;
DI void poll_flags(rsrc_t rws, unsigned voff, int n, u64 tag, int lane, bool& dead) {
  if (dead) return;
  for (unsigned spins = 0;;) {
    u32x2 v = {(unsigned)tag, (unsigned)(tag >> 32)};
    if (lane < n) v = ld64(rws, voff, 0);
    const bool ok = v.x == (unsigned)tag && v.y == (unsigned)(tag >> 32);
    if (__all(ok)) break;
    __builtin_amdgcn_s_sleep(1);
    if (++spins > SPIN_CAP) { dead = true; if (lane == 0) __builtin_amdgcn_raw_buffer_store_b32(1u, rws, WS_ERR, 0, SC1); break; }
  }
}
template <class XL> DI void ensure(bf16x8& x, int s, XL xload, rsrc_t rws, bool& dead) {
  if (__builtin_expect(bad4(__builtin_bit_cast(u32x4, x)), 0)) {
    unsigned spins = 0;
    while (!dead) {
      __builtin_amdgcn_s_sleep(2);
      x = xload(s);
      if (!bad4(__builtin_bit_cast(u32x4, x))) break;
      if (++spins > SPIN_CAP) { dead = true; __builtin_amdgcn_raw_buffer_store_b32(1u, rws, WS_ERR, 0, SC1); }
    }
    __builtin_amdgcn_s_waitcnt(0x0F70);
  }
}
DI void set_flag(rsrc_t rws, unsigned off, u64 tag) { u32x2 v = {(unsigned)tag, (unsigned)(tag >> 32)}; st64(v, rws, off, 0); }

template <int NP> DI unsigned raw_voff(int j, int lane, unsigned rowpitch, unsigned cb0, unsigned cbs) {
  const int beta = 2 * j + (lane >> 5), oct = beta / NP, pair = beta % NP, r = (lane >> 2) & 7, q4 = lane & 3;
  return (unsigned)(8 * oct + r) * rowpitch + cb0 + (unsigned)pair * cbs + (unsigned)((((q4 >> 1) ^ (r >> 2) ^ oct) & 1) * 32 + (q4 & 1) * 16);
}
template <int NP, int ORD> DI bf16x8 raw_frag(const LAS char* kbase, int ct, int lane) {
  const int g = lane >> 4, q = (lane & 15) >> 2, pp = lane & 3, pair = ct >> 1, c = ct & 1;
  int o1, r1, o2, r2;
  if (ORD == 0) { o1 = g >> 1; r1 = 4 * (g & 1) + q; o2 = o1 + 2; r2 = r1; }
  else { o1 = 2 * (g >> 1); r1 = 4 * (g & 1) + q; o2 = o1 + 1; r2 = r1; }
  const s16x4 lo = trd(kbase + (o1 * NP + pair) * 512 + r1 * 64 + ((c ^ (r1 >> 2) ^ o1) & 1) * 32 + pp * 8);
  const s16x4 hi = trd(kbase + (o2 * NP + pair) * 512 + r2 * 64 + ((c ^ (r2 >> 2) ^ o2) & 1) * 32 + pp * 8);
  return __builtin_shufflevector(lo, hi, 0, 1, 2, 3, 4, 5, 6, 7);
}

template <int NT, int S0, int S1, int NX, int PD, bool VAL, class XL, class OFF, class HOOK>
DI void gemm_lds(f32x4 (&acc)[NT], bf16x8 (&X)[NX], const LAS char* wb, XL xload, OFF off, HOOK hook, rsrc_t rws, bool& dead) {
  bf16x8 wf[2][NT];
#pragma unroll
  for (int c = 0; c < NT; ++c) wf[0][c] = ldsfrag(wb + off(S0) + c * 1024);
  if (VAL && S0 == 0) ensure(X[0], 0, xload, rws, dead);
  SBAR();
#pragma unroll
  for (int s = S0; s < S1; ++s) {
    const int cur = (s - S0) & 1;
    if (PD > 0) {
#pragma unroll
      for (int e = 0; e < 2; ++e) { const int ix = PD + 2 * s + e; if (ix < NX) X[ix] = xload(ix); }
    }
    if (VAL && s + 1 < NX) ensure(X[s + 1], s + 1, xload, rws, dead);
    hook(s);
    __builtin_amdgcn_s_waitcnt(0xC07F);
    SBAR();
#pragma unroll
    for (int c = 0; c < NT; ++c) {
      if (s + 1 < S1) wf[cur ^ 1][c] = ldsfrag(wb + off(s + 1) + c * 1024);
      acc[c] = mfma16(wf[cur][c], X[s], acc[c]);
    }
#pragma unroll
    for (int c = 0; c < NT; ++c) {
      __builtin_amdgcn_sched_group_barrier(0x008, 1, 0);
      if (s + 1 < S1 && 2 * c < NT) __builtin_amdgcn_sched_group_barrier(0x100, 2, 0);
    }
    SBAR();
  }
}

#define STAMP(i) do {} while (0)
#define RT(i) do {} while (0)
#define STAMP_OUT() do {} while (0)

DI void stats_load(rsrc_t rws, unsigned base, int lane, u32x2 (&pv)[4]) {
  const int tok = lane & 15, g = lane >> 4;
#pragma unroll
  for (int i = 0; i < 4; ++i) pv[i] = ld64(rws, (unsigned)((4 * g + i) * 128 + tok * 8), base);
}
DI void stats_ensure(rsrc_t rws, unsigned base, int lane, u32x2 (&pv)[4], bool& dead) {
  if (__builtin_expect(__any(umax(umax(umax(pv[0].x, pv[0].y), umax(pv[1].x, pv[1].y)), umax(umax(pv[2].x, pv[2].y), umax(pv[3].x, pv[3].y))) == SENT) && !NOSYNC_, 0)) {
    unsigned spins = 0;
    while (!dead) {
      __builtin_amdgcn_s_sleep(2);
      stats_load(rws, base, lane, pv);
      if (!(bad2(pv[0]) || bad2(pv[1]) || bad2(pv[2]) || bad2(pv[3]))) break;
      if (++spins > SPIN_CAP) { dead = true; __builtin_amdgcn_raw_buffer_store_b32(1u, rws, WS_ERR, 0, SC1); }
    }
    __builtin_amdgcn_s_waitcnt(0x0F70);
  }
}
DI void stats_from_partials(const u32x2 (&pv)[4], float& mean, float& var) {
  float s = 0.f;
#pragma unroll
  for (int i = 0; i < 4; ++i) s += __uint_as_float(pv[i].x);
  s = quad_sum(s);
  mean = s * (1.0f / 512.0f);
  float m2 = 0.f;
#pragma unroll
  for (int i = 0; i < 4; ++i) { const float d = __uint_as_float(pv[i].x) * (1.0f / 32.0f) - mean; m2 += __uint_as_float(pv[i].y) + 32.0f * d * d; }
  m2 = quad_sum(m2);
  var = m2 * (1.0f / 512.0f);
}

DI void publish_x(rsrc_t rws, const float (&v)[8], unsigned xoff, unsigned stoff, int lane, u32x4& packed) {
  packed.x = cvtpk(v[0], v[1]); packed.y = cvtpk(v[2], v[3]); packed.z = cvtpk(v[4], v[5]); packed.w = cvtpk(v[6], v[7]);
  st128(packed, rws, (unsigned)lane * 16u, xoff);
  float r[8] = {bflo(packed.x), bfhi(packed.x), bflo(packed.y), bfhi(packed.y), bflo(packed.z), bfhi(packed.z), bflo(packed.w), bfhi(packed.w)};
  float s = 0.f;
#pragma unroll
  for (int i = 0; i < 8; ++i) s += r[i];
  s = quad_sum(s);
  const float mu = s * (1.0f / 32.0f);
  float m2 = 0.f;
#pragma unroll
  for (int i = 0; i < 8; ++i) { const float d = r[i] - mu; m2 += d * d; }
  m2 = quad_sum(m2);
  if (lane < 16) { u32x2 o = {__float_as_uint(s), __float_as_uint(m2)}; st64(o, rws, (unsigned)lane * 8u, stoff); }
}

template <bool FULL, class HOOK>
DI u32x4 attn_wave(const LAS char* lds, int hd, int qi, int jmin, int lane, HOOK hook) {
  const int ql = lane & 31, h = lane >> 5;
  const bf16x8 Q = ldsfrag(lds + A_Q + (hd * 2 + h) * 2048 + (32 * qi + ql) * 16);
  const LAS char* kb = lds + A_K + (hd * 2 + h) * PLN + (32 * qi + ql) * 16;
  const int G = lane >> 4, q4 = (lane & 15) >> 2, pp = lane & 3;
  const LAS char* vb = lds + ((G & 1) ? A_ONE : A_V + (hd * 2 + (pp >> 1)) * PLN) + (32 * qi + 4 * h + q4) * 16 + (pp & 1) * 8;
  constexpr float NEG = -1e30f;
  const f32x16 zero16 = {0.f,0.f,0.f,0.f,0.f,0.f,0.f,0.f,0.f,0.f,0.f,0.f,0.f,0.f,0.f,0.f};
  f32x16 biasD;
  const int dq = ql - 4 * h;
#pragma unroll
  for (int r = 0; r < 16; ++r) biasD[r] = ((r & 3) + 8 * (r >> 2) <= dq) ? 0.f : NEG;
  float m0 = NEG, m1 = NEG;
  {
    bf16x8 ka = ldsfrag(kb + 8 * 512), kn = ka;
    if (FULL || 7 >= jmin) kn = ldsfrag(kb + 7 * 512);
    f32x16 sa = mfma32(ka, Q, biasD), sb = sa;
    SBAR();
#pragma unroll
    for (int j = 8; j >= 0; --j) {
      f32x16& cur = (j & 1) ? sb : sa; f32x16& nxt = (j & 1) ? sa : sb;
      if (j >= 1 && (FULL || j - 1 >= jmin)) {
        const bf16x8 kj = kn;
        if (j >= 2 && (FULL || j - 2 >= jmin)) kn = ldsfrag(kb + (j - 2) * 512);
        if (j - 1 == 0) { f32x16 bf;
#pragma unroll
          for (int r = 0; r < 16; ++r) bf[r] = NEG - biasD[r];
          nxt = mfma32(kj, Q, bf); }
        else nxt = mfma32(kj, Q, zero16);
      }
      if (FULL || j >= jmin) {
#pragma unroll
        for (int r = 0; r < 16; r += 4) { m0 = max3f(m0, cur[r], cur[r + 1]); m1 = max3f(m1, cur[r + 2], cur[r + 3]); }
        asm volatile("" : "+v"(m0), "+v"(m1));
      }
      SBAR();
    }
  }
  const float m = swap32_max(fmaxf(m0, m1));
  f32x16 negm;
#pragma unroll
  for (int r = 0; r < 16; ++r) negm[r] = -m;
  f32x16 o = zero16;
  {
    bf16x8 ka = ldsfrag(kb + 8 * 512), kn = ka;
    if (FULL || 7 >= jmin) kn = ldsfrag(kb + 7 * 512);
    f32x16 sa = mfma32(ka, Q, biasD + negm), sb = sa;
    SBAR();
#pragma unroll
    for (int j = 8; j >= 0; --j) {
      f32x16& cur = (j & 1) ? sb : sa; f32x16& nxt = (j & 1) ? sa : sb;
      const bool have = FULL || j >= jmin;
      hook(j);
      s16x4 v0 = {}, v1 = {}, v2 = {}, v3 = {};
      if (have) { v0 = trd(vb + (32 * j) * 16); v1 = trd(vb + (32 * j + 8) * 16); v2 = trd(vb + (32 * j + 16) * 16); v3 = trd(vb + (32 * j + 24) * 16); }
      if (j >= 1 && (FULL || j - 1 >= jmin)) {
        const bf16x8 kj = kn;
        if (j >= 2 && (FULL || j - 2 >= jmin)) kn = ldsfrag(kb + (j - 2) * 512);
        if (j - 1 == 0) { f32x16 cf;
#pragma unroll
          for (int r = 0; r < 16; ++r) cf[r] = (NEG - biasD[r]) - m;
          nxt = mfma32(kj, Q, cf); }
        else nxt = mfma32(kj, Q, negm);
      }
      if (have) {
        unsigned pk[8];
#pragma unroll
        for (int r = 0; r < 8; ++r) pk[r] = cvtpk(__builtin_amdgcn_exp2f(cur[2 * r]), __builtin_amdgcn_exp2f(cur[2 * r + 1]));
        o = mfma32(__builtin_shufflevector(v0, v1, 0, 1, 2, 3, 4, 5, 6, 7), asfrag(u32x4{pk[0], pk[1], pk[2], pk[3]}), o);
        o = mfma32(__builtin_shufflevector(v2, v3, 0, 1, 2, 3, 4, 5, 6, 7), asfrag(u32x4{pk[4], pk[5], pk[6], pk[7]}), o);
      }
      SBAR();
    }
  }
  const float inv = __builtin_amdgcn_rcpf(o[8]);
  return u32x4{cvtpk(o[0] * inv, o[1] * inv), cvtpk(o[2] * inv, o[3] * inv), cvtpk(o[4] * inv, o[5] * inv), cvtpk(o[6] * inv, o[7] * inv)};
}

__global__ __launch_bounds__(512, 2) void mega(Params P) {
  __shared__ __attribute__((aligned(16))) char lds_[LDS_BYTES];
  const LAS char* lds = (const LAS char*)lds_;
  LAS char* ldsw = (LAS char*)lds_;
  const unsigned ldsb = (unsigned)(size_t)lds_;
  int w = rfl(threadIdx.x >> 6);
  int lane = (int)__builtin_amdgcn_mbcnt_hi(~0u, __builtin_amdgcn_mbcnt_lo(~0u, 0u));
  const int b = blockIdx.x;
  int t = 4 * ((b & 7) >> 1) + (b >> 6), p = 8 * (b & 1) + ((b >> 3) & 7);
  int hh = (p >> 2) & 1, cg = 4 * (p >> 3) + (p & 3);
  int tt = w & 3, kh = w >> 2;
  int tauA = 8 * t + w, tauB = 8 * t + 4 * hh + tt, ksB = 2 * cg + kh, u32i = 2 * t + hh;
  int tok = lane & 15, g = lane >> 4;
  const rsrc_t rws = __builtin_amdgcn_make_buffer_rsrc(P.ws, 0, (int)WS_END, 0x00020000);
  bool dead = false;
  STAMP(0); RT(59);
  if (w == 7) {
    u64 pc;
    asm volatile("s_getpc_b64 %0" : "=s"(pc));
    const rsrc_t rc = __builtin_amdgcn_make_buffer_rsrc((void*)pc, 0, CODE_BYTES, 0x00020000);
    touch8k(rc, (unsigned)lane * 128u, (unsigned)((b >> 3) % 5) * 8192u, ldsb + T_DUMP);
    touch8k(rws, (unsigned)lane << 20, 0u, ldsb + T_DUMP);
  }
  u64 tag = 0;
  u32x4 resid = {0u, 0u, 0u, 0u};
  auto nohook = [](int) INL {};
  auto noload = [](int) INL { return bf16x8{}; };

  int nL = 2;
  asm volatile("" : "+s"(nL));
#pragma unroll 1
  for (int L = 0; L < nL; ++L) {
    asm volatile("" : "+s"(L));
    const unsigned v16 = (unsigned)lane * 16u;
    auto load_wq = [&](int w0, int nw) INL {
      if (w >= w0) for (int sl = w - w0; sl < 16; sl += nw) { const unsigned src = WS_WQT + (unsigned)((p * 16 + (sl ^ (2 * (p & 7)))) * 6) * 1024u, dst = ldsb + MAIN + (unsigned)sl * 6144u;
        dma4k(rws, (unsigned)lane * 16u, src, dst); dma2k(rws, (unsigned)lane * 16u, src + 4096u, dst + 4096u); }
    };
    auto load_w1 = [&](int h0, int h1, int w0, int nw) INL {
      if (p < NP1 && w >= w0) for (int hs = h0 + w - w0; hs < h1; hs += nw) { const int sl = hs >> 1;
        dma4k(rws, (unsigned)lane * 16u, WS_W1T + (unsigned)((p * 16 + (sl ^ (2 * (p & 7)))) * 8 + (hs & 1) * 4) * 1024u, ldsb + L_W1 + (unsigned)hs * 4096u); }
    };
    auto w2_half = [&](int i, int e) INL {
      const int slot = i >= 19 ? i - 19 : i, k = i ^ (p & 3);
      dma4k(rws, (unsigned)lane * 16u, WS_W2T + (unsigned)((cg * 56 + e * 28 + k) * 4) * 1024u, ldsb + MAIN + (unsigned)(slot * 8 + e * 4) * 1024u);
    };
    auto load_w2 = [&](int i0, int i1, int w0, int nw) INL {
      if (w >= w0) for (int q = 2 * i0 + w - w0; q < 2 * i1; q += nw) w2_half(q >> 1, q & 1);
    };
    (void)v16;
    FRESH();
    {
      bf16x8 X[16];
      float mean = 0.f, var = 0.f;
      const int rotA = 2 * (p & 7);
      f32x4 akv[4];
#pragma unroll
      for (int c = 0; c < 4; ++c) akv[c] = f32x4{0.f, 0.f, 0.f, 0.f};
      if (L == 0) {
        const rsrc_t rx = __builtin_amdgcn_make_buffer_rsrc((void*)P.x, 0, S_ * D_ * 2, 0x00020000);
        if (w < 2) {
          const rsrc_t rq = __builtin_amdgcn_make_buffer_rsrc((void*)P.wqkv, 0, D_ * 3 * D_ * 2, 0x00020000);
#pragma unroll
          for (int jj = 0; jj < 3; ++jj) { const int j = 3 * w + jj; dma16(rq, raw_voff<3>(j, lane, 3072u, (unsigned)p * 64u, 1024u), rfl((unsigned)t * 32u * 3072u), rfl(ldsb + L_PRE + 18 * KB + (unsigned)j * 1024u)); }
        } else if (w < 4) {
          if (p < NP1) {
            const rsrc_t r1 = __builtin_amdgcn_make_buffer_rsrc((void*)P.w1, 0, D_ * F_ * 2, 0x00020000);
#pragma unroll
            for (int jj = 0; jj < 4; ++jj) { const int j = 4 * (w - 2) + jj; dma16(r1, raw_voff<4>(j, lane, 3584u, (unsigned)p * 256u, 64u), rfl((unsigned)t * 32u * 3584u), rfl(ldsb + L_PRE + (unsigned)j * 1024u)); }
          }
        } else if (w == 4) {
          const rsrc_t ro = __builtin_amdgcn_make_buffer_rsrc((void*)P.wo, 0, D_ * D_ * 2, 0x00020000);
#pragma unroll
          for (int j = 0; j < 2; ++j) dma16(ro, raw_voff<1>(j, lane, 1024u, (unsigned)(cg * 128 + (u32i & 1) * 64), 0u), rfl((unsigned)(u32i >> 1) * 32u * 1024u), rfl(ldsb + L_PRE + 8 * KB + (unsigned)j * 1024u));
        } else if (w == 5 || (w == 6 && u32i < 24)) {
          const rsrc_t r2 = __builtin_amdgcn_make_buffer_rsrc((void*)P.w2, 0, F_ * D_ * 2, 0x00020000);
          const int ks = u32i + (w - 5) * 32;
#pragma unroll
          for (int j = 0; j < 4; ++j) dma16(r2, raw_voff<2>(j, lane, 1024u, (unsigned)cg * 128u, 64u), rfl((unsigned)ks * 32u * 1024u), rfl(ldsb + L_PRE + (10 + (w - 5) * 4) * KB + (unsigned)j * 1024u));
        }
#pragma unroll
        for (int r = 0; r < 16; ++r) { const int rr = r ^ (2 * (p & 7)); dma16(rx, (unsigned)lane * 16u, (unsigned)(16 * tauA + rr) * 1024u, ldsb + MAIN + (unsigned)((16 * w + rr) * XROW)); }
        u32x2 epoch_raw = ld64(rws, WS_EPOCH, 0);
        STAMP(23);
        VMWAIT0();
        tag = ((((u64)rfl(epoch_raw.y)) << 32) | rfl(epoch_raw.x)) + 1ull;
        BAR();
        STAMP(1);
        if (w < 6) st128(__builtin_bit_cast(u32x4, raw_frag<3, 0>(lds + L_PRE + 18 * KB, w, lane)), rws, (unsigned)lane * 16u, rfl(WS_WQT + (unsigned)((p * 16 + t) * 6 + w) * 1024u));
        VMWAIT0();
        BAR();
        if (w == 0 && lane == 0) set_flag(rws, FL_WT + (unsigned)(t * 16 + p) * 128u, tag);
        STAMP(2);
        FRESH_V();
        if (p < NP1) st128(__builtin_bit_cast(u32x4, raw_frag<4, 0>(lds + L_PRE, w, lane)), rws, (unsigned)lane * 16u, rfl(WS_W1T + (unsigned)((p * 16 + t) * 8 + w) * 1024u));
        if (w >= 6) st128(__builtin_bit_cast(u32x4, raw_frag<1, 1>(lds + L_PRE + 8 * KB, w - 6, lane)), rws, (unsigned)lane * 16u, rfl(WS_WOT + (unsigned)((cg * 16 + (u32i >> 1)) * 4 + (u32i & 1) * 2 + (w - 6)) * 1024u));
        if (w < 4 || u32i < 24) { const int hs = w >> 2, ks = u32i + hs * 32;
          st128(__builtin_bit_cast(u32x4, raw_frag<2, 0>(lds + L_PRE + (10 + hs * 4) * KB, w & 3, lane)), rws, (unsigned)lane * 16u, rfl(WS_W2T + (unsigned)((cg * 56 + ks) * 4 + (w & 3)) * 1024u)); }
        {
          const LAS char* xp = lds + MAIN + (16 * w + tok) * XROW + g * 8;
#pragma unroll
          for (int s = 0; s < 16; ++s) { const int ks = s ^ rotA; const u32x2 lo = *(const LAS u32x2*)(xp + ks * 64), hi = *(const LAS u32x2*)(xp + ks * 64 + 32); X[s] = asfrag(u32x4{lo.x, lo.y, hi.x, hi.y}); }
          f32x4 s1 = {0.f, 0.f, 0.f, 0.f}, s2 = {0.f, 0.f, 0.f, 0.f};
          const bf16x8 on = ones_frag();
#pragma unroll
          for (int i = 0; i < 16; ++i) { s1 = mfma16(on, X[i], s1); s2 = mfma16(X[i], X[i], s2); }
          mean = s1[0] * (1.0f / 512.0f);
          const int dj = tok - 4 * g;
          const float dv = dj == 0 ? s2[0] : dj == 1 ? s2[1] : dj == 2 ? s2[2] : dj == 3 ? s2[3] : 0.f;
          var = fmaxf(quad_sum(dv) * (1.0f / 512.0f) - mean * mean, 0.f);
        }
        if (w == 0) poll_flags(rws, FL_WT + (unsigned)((lane * 16 + p) * 128), 16, tag, lane, dead);
        BAR();
        STAMP(24);
        load_wq(0, 8);
        unsigned biasv = 0;
        if (w < 2) { if (p < NP1) biasv = P.b1[p * 128 + w * 64 + lane]; } else if (w == 2) biasv = P.b2[cg * 64 + lane];
        {
          const GAS u32x2* rp = (const GAS u32x2*)(P.x + (size_t)(16 * tauB + tok) * D_ + 32 * ksB + 4 * g);
          const u32x2 r0 = rp[0], r1 = rp[4];
          resid = u32x4{r0.x, r0.y, r1.x, r1.y};
        }
        if (w < 3) *(LAS float*)(ldsw + (w < 2 ? T_B1 + (w * 64 + lane) * 4 : T_B2 + lane * 4)) = bf2f(biasv);
        VMWAIT0();
        BAR();
        STAMP(27);
        if (w < 6) {
          f32x4 cs = {0.f, 0.f, 0.f, 0.f};
#pragma unroll
          for (int s = 0; s < 16; ++s) cs = mfma16(ldsfrag(lds + MAIN + (s * 6 + w) * KB + lane * 16), ones_frag(), cs);
          if (tok == 0) *(LAS f32x4*)(ldsw + T_CSQ + (w * 16 + 4 * g) * 4) = cs;
        }
        STAMP(16 + 32 * L);
        gemm_lds<4, 0, 16, 16, 0, false>(akv, X, lds + MAIN + 2 * KB + lane * 16, noload, [](int s) INL { return s * 6 * KB; }, nohook, rws, dead);
        BAR();
      } else {
        STAMP(16 + 32 * L);
        u32x2 pv[4];
        stats_load(rws, WS_ST2 + (unsigned)tauA * 2048u, lane, pv);
        const unsigned xb = WS_X2 + (unsigned)(tauA * 16 + rotA) * 1024u;
        auto xl = [&](int s) INL { return asfrag(ld128(rws, (unsigned)lane * 16u, xb ^ ((unsigned)s << 10))); };
#pragma unroll
        for (int s = 0; s < 8; ++s) X[s] = xl(s);
        stats_from_partials(pv, mean, var);
        gemm_lds<4, 0, 16, 16, 8, false>(akv, X, lds + MAIN + 2 * KB + lane * 16, xl, [](int s) INL { return s * 6 * KB; }, nohook, rws, dead);
      }
      FRESH();
      const float rs = __builtin_amdgcn_rsqf(var + EPS), rm = rs * mean;
      auto qkv_pack = [&](const f32x4& a0, const f32x4& a1, int c0, float sc) INL {
        const f32x4 cs0 = *(const LAS f32x4*)(lds + T_CSQ + (c0 * 16 + 4 * g) * 4), cs1 = *(const LAS f32x4*)(lds + T_CSQ + (c0 * 16 + 16 + 4 * g) * 4);
        float v[8];
#pragma unroll
        for (int j = 0; j < 4; ++j) { v[j] = (rs * a0[j] - rm * cs0[j]) * sc; v[4 + j] = (rs * a1[j] - rm * cs1[j]) * sc; }
        auto s0 = __builtin_amdgcn_permlane16_swap(cvtpk(v[0], v[1]), cvtpk(v[4], v[5]), false, false);
        auto s1 = __builtin_amdgcn_permlane16_swap(cvtpk(v[2], v[3]), cvtpk(v[6], v[7]), false, false);
        return u32x4{s0[0], s1[0], s0[1], s1[1]};
      };
      {
        const u32x4 ko = qkv_pack(akv[0], akv[1], 2, 1.0f), vo = qkv_pack(akv[2], akv[3], 4, 1.0f);
        st128(ko, rws, (unsigned)(PLANE * 2048 + TL * 16), WS_KG + (unsigned)((L * 16 + t) * 16 + p) * 8192u);
        st128(vo, rws, (unsigned)(PLANE * 2048 + TL * 16), WS_VG + (unsigned)((L * 16 + t) * 16 + p) * 8192u);
        *(LAS u32x4*)(ldsw + A_K + PLANE * PLN + (256 + TL) * 16) = ko;
        *(LAS u32x4*)(ldsw + A_V + PLANE * PLN + (256 + TL) * 16) = vo;
      }
      STAMP(3 + 32 * L);
      FRESH();
      f32x4 aq[2] = {f32x4{0.f, 0.f, 0.f, 0.f}, f32x4{0.f, 0.f, 0.f, 0.f}};
      gemm_lds<2, 0, 8, 16, 0, false>(aq, X, lds + MAIN + lane * 16, noload, [](int s) INL { return s * 6 * KB; }, nohook, rws, dead);
      VMWAIT0();
      BAR();
      if (w == 0 && lane == 0) set_flag(rws, flag_row(0, K_KV) + (unsigned)(L * 5 * 256 + t * 16 + p) * 128u, tag);
      gemm_lds<2, 8, 16, 16, 0, false>(aq, X, lds + MAIN + lane * 16, noload, [](int s) INL { return s * 6 * KB; }, nohook, rws, dead);
      STAMP(4 + 32 * L);
      FRESH();
      const u32x4 qo = qkv_pack(aq[0], aq[1], 0, QSCALE);
      if (w == 0) {
        if (L == 0) {
          const int grp = lane >> 4, pg = grp == 0 ? p : 8 * (cg >> 2) + 4 * (grp - 1) + (cg & 3);
          poll_flags(rws, flag_row(0, K_KV) + (unsigned)(((lane & 15) * 16 + pg) * 128), 48, tag, lane, dead);
        } else poll_flags(rws, flag_row(1, K_KV) + (unsigned)(((t - 1 - lane) * 16 + p) * 128), t < 2 ? t : 2, tag, lane, dead);
      }
      BAR();
      STAMP(5 + 32 * L);
      FRESH();
      {
        const int d = 1 + (w >> 2);
        u32x4 hv[4] = {u32x4{}, u32x4{}, u32x4{}, u32x4{}};
        if (t - d >= 0) {
          const unsigned hb = ((w & 2) ? WS_VG : WS_KG) + (unsigned)((L * 16 + t - d) * 16 + p) * 8192u + (unsigned)(w & 1) * 4096u;
#pragma unroll
          for (int i = 0; i < 4; ++i) hv[i] = ld128(rws, (unsigned)lane * 16u, hb + (unsigned)i * 1024u);
        }
        *(LAS u32x4*)(ldsw + A_Q + PLANE * 2048 + TL * 16) = qo;
        if (w * 64 + lane < 400) *(LAS u32x4*)(ldsw + A_ONE + (w * 64 + lane) * 16) = __builtin_bit_cast(u32x4, ones_frag());
        if (t - d >= 0) {
#pragma unroll
          for (int i = 0; i < 4; ++i) *(LAS u32x4*)(ldsw + ((w & 2) ? A_V : A_K) + (2 * (w & 1) + (i >> 1)) * PLN + ((2 - d) * 128 + (i & 1) * 64 + lane) * 16) = hv[i];
        }
      }
      BAR();
      STAMP(6 + 32 * L);
    }
    FRESH();
    {
      if (w >= 4 && p < NP1) touch8k(rws, (unsigned)lane * 128u, WS_W1T + (unsigned)(p * 128 + (t & 3) * 32 + (w - 4) * 8) * 1024u, ldsb + T_DUMP);
      STAMP(17 + 32 * L);
      u32x4 ov;
      if (t >= 2) ov = attn_wave<true>(lds, w & 1, w >> 1, 0, lane, nohook); else ov = attn_wave<false>(lds, w & 1, w >> 1, 8 - 4 * t - (w >> 1), lane, nohook);
      FRESH();
      st128(ov, rws, (unsigned)(((lane & 31) >> 4) * 16384 + ((2 * (w & 1) + (lane >> 5)) * 16 + (lane & 15)) * 16), WS_OG + (unsigned)((L * 128 + 8 * t + 2 * (w >> 1)) * 16 + p) * 1024u);
      STAMP(18 + 32 * L);
      if (w < 4) {
#pragma unroll
        for (int i = 0; i < 4; ++i) { const int sl = w + 4 * i; dma4k(rws, (unsigned)lane * 16u, WS_WOT + (unsigned)((cg * 16 + (sl ^ (2 * (p & 3)))) * 4) * 1024u, ldsb + L_WO + (unsigned)sl * 4096u); }
      }
      VMWAIT0();
      BAR();
      if (w == 0 && lane == 0) set_flag(rws, flag_row(0, K_O) + (unsigned)(L * 5 * 256 + t * 16 + p) * 128u, tag);
      STAMP(7 + 32 * L);
      load_w1(14, 32, 1, 7);
      if (w == 0) poll_flags(rws, flag_row(0, K_O) + (unsigned)(L * 5 * 256 + t * 16 + lane) * 128u, 16, tag, lane, dead);
      BAR();
      STAMP(8 + 32 * L);
    }
    FRESH();
    {
      bf16x8 Xo[8];
      const unsigned xb = WS_OG + (unsigned)((L * 128 + tauB) * 16 + 8 * kh + 2 * (p & 3)) * 1024u;
      auto xl = [&](int i) INL { return asfrag(ld128(rws, (unsigned)lane * 16u, xb ^ ((unsigned)i << 10))); };
#pragma unroll
      for (int i = 0; i < 8; ++i) Xo[i] = xl(i);
      f32x4 acc[4];
#pragma unroll
      for (int c = 0; c < 4; ++c) acc[c] = f32x4{0.f, 0.f, 0.f, 0.f};
      gemm_lds<4, 0, 8, 8, 0, false>(acc, Xo, lds + L_WO + kh * 32 * KB + lane * 16, xl, [](int s) INL { return s * 4 * KB; }, nohook, rws, dead);
      STAMP(19 + 32 * L);
      FRESH();
      *(LAS f32x4*)(ldsw + MAIN + (w * 2 + 0) * KB + lane * 16) = kh ? acc[0] : acc[2];
      *(LAS f32x4*)(ldsw + MAIN + (w * 2 + 1) * KB + lane * 16) = kh ? acc[1] : acc[3];
      BAR();
      const f32x4 o0 = *(const LAS f32x4*)(lds + MAIN + ((w ^ 4) * 2 + 0) * KB + lane * 16), o1 = *(const LAS f32x4*)(lds + MAIN + ((w ^ 4) * 2 + 1) * KB + lane * 16);
      const f32x4 k0 = kh ? acc[2] : acc[0], k1 = kh ? acc[3] : acc[1];
      float v[8];
      v[0] = k0[0] + o0[0] + bflo(resid.x); v[1] = k0[1] + o0[1] + bfhi(resid.x); v[2] = k0[2] + o0[2] + bflo(resid.y); v[3] = k0[3] + o0[3] + bfhi(resid.y);
      v[4] = k1[0] + o1[0] + bflo(resid.z); v[5] = k1[1] + o1[1] + bfhi(resid.z); v[6] = k1[2] + o1[2] + bflo(resid.w); v[7] = k1[3] + o1[3] + bfhi(resid.w);
      publish_x(rws, v, WS_X1 + (unsigned)((L * 128 + tauB) * 16 + ksB) * 1024u, WS_ST1 + (unsigned)((L * 128 + tauB) * 16 + ksB) * 128u, lane, resid);
      VMWAIT0();
      BAR();
      if (w == 0 && lane == 0) set_flag(rws, flag_row(0, K_X1) + (unsigned)(L * 5 * 256 + t * 16 + p) * 128u, tag);
      STAMP(9 + 32 * L);
      load_w1(0, 14, 1, 7);
      if (w >= 4) touch8k(rws, (unsigned)lane * 128u, WS_W2T + (unsigned)(cg * 224 + ((t & 3) * 2 + hh) * 28 + (w - 4) * 8) * 1024u, ldsb + T_DUMP);
      if (w == 0) poll_flags(rws, flag_row(0, K_X1) + (unsigned)(L * 5 * 256 + t * 16 + lane) * 128u, 16, tag, lane, dead);
      if (w >= 4) asm volatile("s_waitcnt vmcnt(1)" ::: "memory"); else VMWAIT0();
      BAR();
      STAMP(10 + 32 * L);
    }
    FRESH();
    if (p < NP1) {
      bf16x8 X1[16];
      u32x2 pv1[4];
      stats_load(rws, WS_ST1 + (unsigned)(L * 128 + tauA) * 2048u, lane, pv1);
      const unsigned xb = WS_X1 + (unsigned)((L * 128 + tauA) * 16 + 2 * (p & 7)) * 1024u;
      auto xl = [&](int s) INL { return asfrag(ld128(rws, (unsigned)lane * 16u, xb ^ ((unsigned)s << 10))); };
#pragma unroll
      for (int s = 0; s < 8; ++s) X1[s] = xl(s);
      if (L == 0) {
        f32x4 cs = {0.f, 0.f, 0.f, 0.f};
#pragma unroll
        for (int s = 0; s < 16; ++s) cs = mfma16(ldsfrag(lds + L_W1 + (s * 8 + w) * KB + lane * 16), ones_frag(), cs);
        if (tok == 0) *(LAS f32x4*)(ldsw + T_CS1 + (w * 16 + 4 * g) * 4) = cs;
      }
      float mean1, var1;
      stats_from_partials(pv1, mean1, var1);
      STAMP(26 + 32 * L);
      f32x4 acc[8];
#pragma unroll
      for (int c = 0; c < 8; ++c) acc[c] = f32x4{0.f, 0.f, 0.f, 0.f};
      auto hook = [&](int s) INL {
        if (s == 13 && w < 6) w2_half(w >> 1, w & 1);
        if (s == 14 && L == 0 && w < 3) touch8k(rws, (unsigned)lane * 128u, WS_WQT + (unsigned)(p * 96 + (t & 3) * 24 + w * 8) * 1024u, ldsb + T_DUMP);
      };
      gemm_lds<8, 0, 16, 16, 8, false>(acc, X1, lds + L_W1 + lane * 16, xl, [](int s) INL { return s * 8 * KB; }, hook, rws, dead);
      BAR();
      STAMP(11 + 32 * L);
      FRESH_V();
      const float r1 = __builtin_amdgcn_rsqf(var1 + EPS), r2 = __builtin_amdgcn_rsqf(var1 * r1 * r1 + EPS), rs = r1 * r2, rm = rs * mean1;
      constexpr float C1 = -2.0f * 0.7978845608028654f * 1.4426950408889634f, C2 = C1 * 0.044715f;
      const f32x2 rs2 = {rs, rs}, nrm2 = {-rm, -rm}, c1v = {C1, C1}, c2v = {C2, C2}, one2 = {1.0f, 1.0f};
#pragma unroll
      for (int u = 0; u < 4; ++u) {
        f32x2 av[4], e[4];
#pragma unroll
        for (int hf = 0; hf < 2; ++hf) {
          const int c = 2 * u + hf;
          const f32x4 cs = *(const LAS f32x4*)(lds + T_CS1 + (c * 16 + 4 * g) * 4), bb = *(const LAS f32x4*)(lds + T_B1 + (c * 16 + 4 * g) * 4);
          av[2 * hf] = rs2 * f32x2{acc[c][0], acc[c][1]} + (nrm2 * f32x2{cs[0], cs[1]} + f32x2{bb[0], bb[1]});
          av[2 * hf + 1] = rs2 * f32x2{acc[c][2], acc[c][3]} + (nrm2 * f32x2{cs[2], cs[3]} + f32x2{bb[2], bb[3]});
        }
#pragma unroll
        for (int i = 0; i < 4; ++i) e[i] = av[i] * (c2v * (av[i] * av[i]) + c1v);
#pragma unroll
        for (int i = 0; i < 4; ++i) e[i] = f32x2{__builtin_amdgcn_exp2f(e[i].x), __builtin_amdgcn_exp2f(e[i].y)} + one2;
#pragma unroll
        for (int i = 0; i < 4; ++i) e[i] = av[i] * f32x2{__builtin_amdgcn_rcpf(e[i].x), __builtin_amdgcn_rcpf(e[i].y)};
        st128(u32x4{cvtpk2(e[0]), cvtpk2(e[1]), cvtpk2(e[2]), cvtpk2(e[3])}, rws, (unsigned)lane * 16u, WS_HG + (unsigned)((L * 128 + tauA) * 56 + 4 * p + u) * 1024u);
        SBAR();
        w2_half(3 + 4 * u + (w >> 1), w & 1);
        SBAR();
      }
      STAMP(20 + 32 * L);
      asm volatile("s_waitcnt vmcnt(4)" ::: "memory");
      BAR();
      if (w == 0 && lane == 0) set_flag(rws, flag_row(0, K_H) + (unsigned)(L * 5 * 256 + t * 16 + p) * 128u, tag);
    } else {
      if (L == 1 && b == 255 && w == 0) {
        bool d2 = dead;
#pragma unroll 1
        for (int i = 0; i < 4; ++i) poll_flags(rws, FL_WT + (unsigned)(i * 64 + lane) * 128u, 64, tag, lane, d2);
        if (lane == 0) set_flag(rws, WS_EPOCH, tag);
      }
      load_w2(0, 19, 0, 8);
    }
    FRESH();
    {
      STAMP(12 + 32 * L);
      if (w == 0) poll_flags(rws, flag_row(0, K_H) + (unsigned)(L * 5 * 256 + t * 16 + lane) * 128u, NP1, tag, lane, dead);
      VMWAIT0();
      BAR();
      STAMP(13 + 32 * L);
      bf16x8 Xh[28];
      const unsigned xb = WS_HG + (unsigned)((L * 128 + tauB) * 56 + 28 * kh + (p & 3)) * 1024u;
      auto xl = [&](int i) INL { return asfrag(ld128(rws, (unsigned)lane * 16u, (xb ^ ((unsigned)(i & 3) << 10)) + (unsigned)(i & ~3) * 1024u)); };
#pragma unroll
      for (int i = 0; i < 10; ++i) Xh[i] = xl(i);
      f32x4 acc[4];
#pragma unroll
      for (int c = 0; c < 4; ++c) acc[c] = f32x4{0.f, 0.f, 0.f, 0.f};
      gemm_lds<4, 0, 9, 28, 10, false>(acc, Xh, lds + MAIN + kh * 4 * KB + lane * 16, xl, [](int s) INL { return s * 8 * KB; }, nohook, rws, dead);
      STAMP(21 + 32 * L);
      BAR();
      load_w2(19, 28, 0, 8);
      gemm_lds<4, 9, 19, 28, 10, false>(acc, Xh, lds + MAIN + kh * 4 * KB + lane * 16, xl, [](int s) INL { return s * 8 * KB; }, nohook, rws, dead);
      VMWAIT0();
      BAR();
      STAMP(22 + 32 * L);
      gemm_lds<4, 19, 28, 28, 10, false>(acc, Xh, lds + MAIN + kh * 4 * KB + lane * 16, xl, [](int s) INL { return (s - 19) * 8 * KB; }, nohook, rws, dead);
      STAMP(14 + 32 * L);
      FRESH();
      *(LAS f32x4*)(ldsw + L_RED2 + (w * 2 + 0) * KB + lane * 16) = kh ? acc[0] : acc[2];
      *(LAS f32x4*)(ldsw + L_RED2 + (w * 2 + 1) * KB + lane * 16) = kh ? acc[1] : acc[3];
      BAR();
      const f32x4 o0 = *(const LAS f32x4*)(lds + L_RED2 + ((w ^ 4) * 2 + 0) * KB + lane * 16), o1 = *(const LAS f32x4*)(lds + L_RED2 + ((w ^ 4) * 2 + 1) * KB + lane * 16);
      const f32x4 k0 = kh ? acc[2] : acc[0], k1 = kh ? acc[3] : acc[1];
      const f32x4 b0 = *(const LAS f32x4*)(lds + T_B2 + (kh * 32 + 4 * g) * 4), b1v = *(const LAS f32x4*)(lds + T_B2 + (kh * 32 + 16 + 4 * g) * 4);
      float v[8];
      v[0] = k0[0] + o0[0] + b0[0] + bflo(resid.x); v[1] = k0[1] + o0[1] + b0[1] + bfhi(resid.x); v[2] = k0[2] + o0[2] + b0[2] + bflo(resid.y); v[3] = k0[3] + o0[3] + b0[3] + bfhi(resid.y);
      v[4] = k1[0] + o1[0] + b1v[0] + bflo(resid.z); v[5] = k1[1] + o1[1] + b1v[1] + bfhi(resid.z); v[6] = k1[2] + o1[2] + b1v[2] + bflo(resid.w); v[7] = k1[3] + o1[3] + b1v[3] + bfhi(resid.w);
      if (L == 1) {
        const unsigned a0 = cvtpk(v[0], v[1]), a1 = cvtpk(v[2], v[3]), c0 = cvtpk(v[4], v[5]), c1 = cvtpk(v[6], v[7]);
        auto s0 = __builtin_amdgcn_permlane16_swap(a0, c0, false, false);
        auto s1 = __builtin_amdgcn_permlane16_swap(a1, c1, false, false);
        const u32x4 o = {s0[0], s1[0], s0[1], s1[1]};
        const int choff = (g & 1) * 16 + (g >> 1) * 8;
        *(GAS u32x4*)(P.out + (size_t)(16 * tauB + tok) * D_ + 32 * ksB + choff) = o;
        STAMP(15 + 32 * L); RT(60); STAMP_OUT();
        break;
      }
      publish_x(rws, v, WS_X2 + (unsigned)(tauB * 16 + ksB) * 1024u, WS_ST2 + (unsigned)(tauB * 16 + ksB) * 128u, lane, resid);
      VMWAIT0();
      BAR();
      if (w == 0 && lane == 0) set_flag(rws, flag_row(0, K_X2) + (unsigned)(t * 16 + p) * 128u, tag);
      load_wq(1, 7);
      if (w == 0) poll_flags(rws, flag_row(0, K_X2) + (unsigned)(t * 16 + lane) * 128u, 16, tag, lane, dead);
      VMWAIT0();
      BAR();
      STAMP(15 + 32 * L);
    }
  }
}

extern "C" void kernel_launch(void* const* d_in, const int* in_sizes, int n_in, void* d_out, int out_size, void* d_ws, size_t ws_size, hipStream_t stream) {
  Params P;
  P.x = (const u16*)d_in[0]; P.wqkv = (const u16*)d_in[1]; P.wo = (const u16*)d_in[2];
  P.w1 = (const u16*)d_in[4]; P.b1 = (const u16*)d_in[5]; P.w2 = (const u16*)d_in[6]; P.b2 = (const u16*)d_in[7];
  P.out = (u16*)d_out; P.ws = (unsigned char*)d_ws;
  mega<<<256, 512, 0, stream>>>(P);
}
```

```cpp
#include <hip/hip_runtime.h>
#include <stdint.h>

typedef unsigned short u16;
typedef unsigned long long u64;
typedef short bf16x8 __attribute__((ext_vector_type(8)));
typedef short s16x4 __attribute__((ext_vector_type(4)));
typedef float f32x2 __attribute__((ext_vector_type(2)));
typedef float f32x4 __attribute__((ext_vector_type(4)));
typedef float f32x16 __attribute__((ext_vector_type(16)));
typedef unsigned u32x4 __attribute__((ext_vector_type(4)));
typedef unsigned u32x2 __attribute__((ext_vector_type(2)));
typedef __bf16 bf16x2_t __attribute__((ext_vector_type(2)));
#define LAS __attribute__((address_space(3)))
#define GAS __attribute__((address_space(1)))
#define DI __device__ __forceinline__
#define INL __attribute__((always_inline))
typedef __amdgpu_buffer_rsrc_t rsrc_t;

constexpr int S_ = 2048, D_ = 512, F_ = 1792;
constexpr float EPS = 1e-5f;
constexpr float QSCALE = 0.25f * 1.4426950408889634f;
constexpr int NP1 = 14;

constexpr unsigned WS_EPOCH = 0, WS_ERR = 128, WS_FLAGS = 4096;
enum { K_KV = 0, K_O = 1, K_X1 = 2, K_H = 3, K_X2 = 4 };
constexpr unsigned flag_row(int L, int kind) { return WS_FLAGS + (unsigned)((L * 5 + kind) * 256 * 128); }
constexpr unsigned FL_WT = flag_row(1, K_X2);
constexpr unsigned WS_WQT = 0x60000;
constexpr unsigned WS_WOT = WS_WQT + 16 * 96 * 1024;
constexpr unsigned WS_W1T = WS_WOT + 8 * 64 * 1024;
constexpr unsigned WS_W2T = WS_W1T + NP1 * 128 * 1024;
constexpr unsigned WS_KG = WS_W2T + 8 * 224 * 1024;
constexpr unsigned WS_VG = WS_KG + 2 * 256 * 8192;
constexpr unsigned WS_OG = WS_VG + 2 * 256 * 8192;
constexpr unsigned WS_X1 = WS_OG + 2 * 128 * 16 * 1024;
constexpr unsigned WS_ST1 = WS_X1 + 2 * 128 * 16 * 1024;
constexpr unsigned WS_HG = WS_ST1 + 2 * 128 * 16 * 128;
constexpr unsigned WS_X2 = WS_HG + 2 * 128 * 56 * 1024;
constexpr unsigned WS_ST2 = WS_X2 + 128 * 16 * 1024;
constexpr unsigned WS_DBG = WS_ST2 + 128 * 16 * 128;
constexpr unsigned WS_END = WS_DBG + 256 * 8 * 128 * 8;

constexpr int KB = 1024;
constexpr int CODE_BYTES = 36 * 1024;
constexpr int LDS_BYTES = 163840;
constexpr int T_CSQ = 0;
constexpr int T_CS1 = 512;
constexpr int T_B1 = 1024;
constexpr int T_B2 = 1536;
constexpr int T_DUMP = 1792;
constexpr int MAIN = 2048;
constexpr int PLN = 384 * 16 + 64;
constexpr int A_K = MAIN + 96 * KB, A_V = A_K + 4 * PLN, A_Q = MAIN, A_ONE = A_Q + 4 * 2048;
static_assert(A_V + 4 * PLN <= LDS_BYTES && A_ONE + 6400 <= MAIN + 16 * KB, "attention buffers");
constexpr int T_MASK = MAIN + 148 * KB;
static_assert(A_V + 4 * PLN <= T_MASK && T_MASK + 4096 <= LDS_BYTES, "mask table");
constexpr int L_W1 = MAIN + 30 * KB;
constexpr int L_WO = MAIN + 16 * KB;
constexpr int L_PRE = MAIN + 132 * KB;
constexpr int XROW = 1040;
static_assert(128 * XROW <= 132 * KB && L_PRE + 24 * KB <= LDS_BYTES, "prologue scratch");
constexpr int L_RED2 = MAIN + 96 * KB;
static_assert(L_W1 + 128 * KB <= LDS_BYTES && L_WO + 64 * KB <= LDS_BYTES, "images");

template <int V> struct IC { static constexpr int value = V; };
struct Params {
  const u16 *x, *wqkv, *wo, *w1, *b1, *w2, *b2;
  u16* out;
  unsigned char* ws;
};

DI float bf2f(unsigned v) { return __uint_as_float(v << 16); }
DI float bflo(unsigned v) { return __uint_as_float(v << 16); }
DI float bfhi(unsigned v) { return __uint_as_float(v & 0xffff0000u); }
DI unsigned cvtpk(float lo, float hi) { f32x2 v = {lo, hi}; bf16x2_t b = __builtin_convertvector(v, bf16x2_t); return __builtin_bit_cast(unsigned, b); }
DI unsigned cvtpk2(f32x2 v) { bf16x2_t b = __builtin_convertvector(v, bf16x2_t); return __builtin_bit_cast(unsigned, b); }
DI f32x4 mfma16(bf16x8 a, bf16x8 b, f32x4 c) { return __builtin_amdgcn_mfma_f32_16x16x32_bf16(a, b, c, 0, 0, 0); }
DI f32x16 mfma32(bf16x8 a, bf16x8 b, f32x16 c) { return __builtin_amdgcn_mfma_f32_32x32x16_bf16(a, b, c, 0, 0, 0); }
DI bf16x8 ones_frag() { unsigned o = 0x3F803F80u; asm volatile("" : "+v"(o)); return __builtin_bit_cast(bf16x8, u32x4{o, o, o, o}); }
DI bf16x8 asfrag(u32x4 v) { return __builtin_bit_cast(bf16x8, v); }
DI s16x4 trd(const LAS char* p) { return __builtin_bit_cast(s16x4, __builtin_amdgcn_ds_read_tr16_b64_v4i16((LAS s16x4*)p)); }
DI bf16x8 ldsfrag(const LAS char* p) { return *(const LAS bf16x8*)p; }
DI float max3f(float a, float b, float c) { return __builtin_fmaxf(__builtin_fmaxf(a, b), c); }
DI float swap32_add(float x) { auto r = __builtin_amdgcn_permlane32_swap(__float_as_uint(x), __float_as_uint(x), false, false); return __uint_as_float(r[0]) + __uint_as_float(r[1]); }
DI float swap16_add(float x) { auto r = __builtin_amdgcn_permlane16_swap(__float_as_uint(x), __float_as_uint(x), false, false); return __uint_as_float(r[0]) + __uint_as_float(r[1]); }
DI float quad_sum(float x) { return swap32_add(swap16_add(x)); }
DI float swap32_max(float x) { auto r = __builtin_amdgcn_permlane32_swap(__float_as_uint(x), __float_as_uint(x), false, false); return fmaxf(__uint_as_float(r[0]), __uint_as_float(r[1])); }

#define FRESH() do { asm volatile("" : "+v"(lane), "+s"(w), "+s"(t), "+s"(p)); tok = lane & 15; g = lane >> 4; hh = p >> 3; cg = p & 7; tt = w & 3; kh = w >> 2; \
  tauA = 8 * t + w; tauB = 8 * t + 4 * hh + tt; ksB = 2 * cg + kh; u32i = 2 * t + hh; } while (0)
#define PLANE ((g & 1) * 2 + (g >> 1))
#define TL (16 * w + tok)
#define FRESH_V() do { asm volatile("" : "+v"(lane)); tok = lane & 15; g = lane >> 4; } while (0)
#define BAR() asm volatile("s_waitcnt lgkmcnt(0)\n\ts_barrier" ::: "memory")
#define VMWAIT0() asm volatile("s_waitcnt vmcnt(0)" ::: "memory")
#define SBAR() __builtin_amdgcn_sched_barrier(0)
constexpr int SC1 = 16;
#define OUT_AUX 16
#define XAUX 16
DI u32x4 ld128(rsrc_t r, unsigned voff, unsigned soff) { return __builtin_amdgcn_raw_buffer_load_b128(r, voff, soff, XAUX); }
DI u32x2 ld64(rsrc_t r, unsigned voff, unsigned soff) { return __builtin_amdgcn_raw_buffer_load_b64(r, voff, soff, SC1); }
DI void st128(u32x4 v, rsrc_t r, unsigned voff, unsigned soff) { __builtin_amdgcn_raw_buffer_store_b128(v, r, voff + soff, 0, SC1); }
DI void st64(u32x2 v, rsrc_t r, unsigned voff, unsigned soff) { __builtin_amdgcn_raw_buffer_store_b64(v, r, voff + soff, 0, SC1); }
DI unsigned rfl(unsigned x) { return __builtin_amdgcn_readfirstlane(x); }
DI void dma16(rsrc_t r, unsigned voff, unsigned soff, unsigned ldsaddr) {
  asm volatile("s_mov_b32 m0, %0\n\ts_nop 3\n\tbuffer_load_dwordx4 %1, %2, %3 offen lds" :: "s"(rfl(ldsaddr)), "v"(voff), "s"(r), "s"(rfl(soff)) : "memory");
}
DI void dma16_sc1(rsrc_t r, unsigned voff, unsigned soff, unsigned ldsaddr) {
  asm volatile("s_mov_b32 m0, %0\n\ts_nop 3\n\tbuffer_load_dwordx4 %1, %2, %3 offen sc1 lds" :: "s"(rfl(ldsaddr)), "v"(voff), "s"(r), "s"(rfl(soff)) : "memory");
}
DI void dma4k(rsrc_t r, unsigned voff, unsigned soff, unsigned ldsaddr) {
  asm volatile("s_mov_b32 m0, %0\n\ts_nop 3\n\tbuffer_load_dwordx4 %1, %2, %3 offen sc1 lds\n\tbuffer_load_dwordx4 %1, %2, %3 offen offset:1024 sc1 lds\n\t"
               "buffer_load_dwordx4 %1, %2, %3 offen offset:2048 sc1 lds\n\tbuffer_load_dwordx4 %1, %2, %3 offen offset:3072 sc1 lds" :: "s"(rfl(ldsaddr)), "v"(voff), "s"(r), "s"(rfl(soff)) : "memory");
}
DI void dma2k(rsrc_t r, unsigned voff, unsigned soff, unsigned ldsaddr) {
  asm volatile("s_mov_b32 m0, %0\n\ts_nop 3\n\tbuffer_load_dwordx4 %1, %2, %3 offen sc1 lds\n\tbuffer_load_dwordx4 %1, %2, %3 offen offset:1024 sc1 lds" :: "s"(rfl(ldsaddr)), "v"(voff), "s"(r), "s"(rfl(soff)) : "memory");
}
DI void touch8k(rsrc_t r, unsigned voff, unsigned soff, unsigned ldsdump) {
  asm volatile("s_mov_b32 m0, %0\n\ts_nop 3\n\tbuffer_load_dword %1, %2, %3 offen sc1 lds" :: "s"(rfl(ldsdump)), "v"(voff), "s"(r), "s"(rfl(soff)) : "memory");
}

constexpr unsigned SENT = 0xFFFFFFFFu;
constexpr bool NOSYNC_ = false;
DI unsigned san(unsigned v) { return v < SENT - 1u ? v : SENT - 1u; }
DI u32x4 san4(u32x4 v) { return u32x4{san(v.x), san(v.y), san(v.z), san(v.w)}; }
DI unsigned umax(unsigned a, unsigned b) { return a > b ? a : b; }
DI bool bad4(u32x4 v) {
  return __any(umax(umax(v.x, v.y), umax(v.z, v.w)) == SENT); }
DI bool bad2(u32x2 v) {
  return __any(umax(v.x, v.y) == SENT); }
#define POLL_GAP 3
constexpr unsigned SPIN_CAP = 1u << 14;
DI void poll_flags(rsrc_t rws, unsigned voff, int n, u64 tag, int lane, bool& dead) {
  if (dead) return;
  const unsigned vo = lane < n ? voff : WS_EPOCH + 64u;
  const bool idle = lane >= n;
  u32x2 v0, v1, v2;
#define POLL_ISSUE(v) asm volatile("buffer_load_dwordx2 %0, %1, %2, 0 offen sc1" : "=v"(v) : "v"(vo), "s"(rws) : "memory")
#define POLL_OK(v) __all(idle || (v.x == (unsigned)tag && v.y == (unsigned)(tag >> 32)))
  asm volatile("s_waitcnt vmcnt(0)" ::: "memory");
  POLL_ISSUE(v0); __builtin_amdgcn_s_sleep(POLL_GAP);
  POLL_ISSUE(v1); __builtin_amdgcn_s_sleep(POLL_GAP);
  POLL_ISSUE(v2);
  for (unsigned spins = 0;;) {
    asm volatile("s_waitcnt vmcnt(2)" : "+v"(v0) :: "memory");
    if (POLL_OK(v0)) break;
    POLL_ISSUE(v0);
    asm volatile("s_waitcnt vmcnt(2)" : "+v"(v1) :: "memory");
    if (POLL_OK(v1)) break;
    POLL_ISSUE(v1);
    asm volatile("s_waitcnt vmcnt(2)" : "+v"(v2) :: "memory");
    if (POLL_OK(v2)) break;
    POLL_ISSUE(v2);
    if (++spins > SPIN_CAP) { dead = true; break; }
  }
  asm volatile("s_waitcnt vmcnt(0)" : "+v"(v0), "+v"(v1), "+v"(v2) :: "memory");
  if (dead && lane == 0) __builtin_amdgcn_raw_buffer_store_b32(1u, rws, WS_ERR, 0, SC1);
#undef POLL_ISSUE
#undef POLL_OK
}
template <class XL> DI void ensure(bf16x8& x, int s, XL xload, rsrc_t rws, bool& dead) {
  if (__builtin_expect(bad4(__builtin_bit_cast(u32x4, x)), 0)) {
    unsigned spins = 0;
    while (!dead) {
      __builtin_amdgcn_s_sleep(2);
      x = xload(s);
      if (!bad4(__builtin_bit_cast(u32x4, x))) break;
      if (++spins > SPIN_CAP) { dead = true; __builtin_amdgcn_raw_buffer_store_b32(1u, rws, WS_ERR, 0, SC1); }
    }
    __builtin_amdgcn_s_waitcnt(0x0F70);
  }
}
DI void set_flag(rsrc_t rws, unsigned off, u64 tag) { u32x2 v = {(unsigned)tag, (unsigned)(tag >> 32)}; st64(v, rws, off, 0); }

template <int NP> DI unsigned raw_voff(int j, int lane, unsigned rowpitch, unsigned cb0, unsigned cbs) {
  const int beta = 2 * j + (lane >> 5), oct = beta / NP, pair = beta % NP, r = (lane >> 2) & 7, q4 = lane & 3;
  return (unsigned)(8 * oct + r) * rowpitch + cb0 + (unsigned)pair * cbs + (unsigned)((((q4 >> 1) ^ (r >> 2) ^ oct) & 1) * 32 + (q4 & 1) * 16);
}
template <int NP, int ORD> DI bf16x8 raw_frag(const LAS char* kbase, int ct, int lane) {
  const int g = lane >> 4, q = (lane & 15) >> 2, pp = lane & 3, pair = ct >> 1, c = ct & 1;
  int o1, r1, o2, r2;
  if (ORD == 0) { o1 = g >> 1; r1 = 4 * (g & 1) + q; o2 = o1 + 2; r2 = r1; }
  else { o1 = 2 * (g >> 1); r1 = 4 * (g & 1) + q; o2 = o1 + 1; r2 = r1; }
  const s16x4 lo = trd(kbase + (o1 * NP + pair) * 512 + r1 * 64 + ((c ^ (r1 >> 2) ^ o1) & 1) * 32 + pp * 8);
  const s16x4 hi = trd(kbase + (o2 * NP + pair) * 512 + r2 * 64 + ((c ^ (r2 >> 2) ^ o2) & 1) * 32 + pp * 8);
  return __builtin_shufflevector(lo, hi, 0, 1, 2, 3, 4, 5, 6, 7);
}

template <int NT, int S0, int S1, int NX, int PD, bool VAL, class XL, class OFF, class HOOK>
DI void gemm_lds(f32x4 (&acc)[NT], bf16x8 (&X)[NX], const LAS char* wb, XL xload, OFF off, HOOK hook, rsrc_t rws, bool& dead) {
  bf16x8 wf[2][NT];
#pragma unroll
  for (int c = 0; c < NT; ++c) wf[0][c] = ldsfrag(wb + off(S0) + c * 1024);
  if (VAL && S0 == 0) ensure(X[0], 0, xload, rws, dead);
  SBAR();
#pragma unroll
  for (int s = S0; s < S1; ++s) {
    const int cur = (s - S0) & 1;
    if (PD > 0) {
#pragma unroll
      for (int e = 0; e < 2; ++e) { const int ix = PD + 2 * s + e; if (ix < NX) X[ix] = xload(ix); }
    }
    if (VAL && s + 1 < NX) ensure(X[s + 1], s + 1, xload, rws, dead);
    hook(s);
    __builtin_amdgcn_s_waitcnt(0xC07F);
    SBAR();
#pragma unroll
    for (int c = 0; c < NT; ++c) {
      if (s + 1 < S1) wf[cur ^ 1][c] = ldsfrag(wb + off(s + 1) + c * 1024);
      acc[c] = mfma16(wf[cur][c], X[s], acc[c]);
    }
#pragma unroll
    for (int c = 0; c < NT; ++c) {
      __builtin_amdgcn_sched_group_barrier(0x008, 1, 0);
      if (s + 1 < S1 && 2 * c < NT) __builtin_amdgcn_sched_group_barrier(0x100, 2, 0);
    }
    SBAR();
  }
}

#define STAMP(i) do {} while (0)
#define RT(i) do {} while (0)
#define STAMP_OUT() do {} while (0)

DI void stats_load(rsrc_t rws, unsigned base, int lane, u32x2 (&pv)[4]) {
  const int tok = lane & 15, g = lane >> 4;
#pragma unroll
  for (int i = 0; i < 4; ++i) pv[i] = ld64(rws, (unsigned)((4 * g + i) * 128 + tok * 8), base);
}
DI void stats_ensure(rsrc_t rws, unsigned base, int lane, u32x2 (&pv)[4], bool& dead) {
  if (__builtin_expect(__any(umax(umax(umax(pv[0].x, pv[0].y), umax(pv[1].x, pv[1].y)), umax(umax(pv[2].x, pv[2].y), umax(pv[3].x, pv[3].y))) == SENT) && !NOSYNC_, 0)) {
    unsigned spins = 0;
    while (!dead) {
      __builtin_amdgcn_s_sleep(2);
      stats_load(rws, base, lane, pv);
      if (!(bad2(pv[0]) || bad2(pv[1]) || bad2(pv[2]) || bad2(pv[3]))) break;
      if (++spins > SPIN_CAP) { dead = true; __builtin_amdgcn_raw_buffer_store_b32(1u, rws, WS_ERR, 0, SC1); }
    }
    __builtin_amdgcn_s_waitcnt(0x0F70);
  }
}
DI void stats_from_partials(const u32x2 (&pv)[4], float& mean, float& var) {
  float s = 0.f;
#pragma unroll
  for (int i = 0; i < 4; ++i) s += __uint_as_float(pv[i].x);
  s = quad_sum(s);
  mean = s * (1.0f / 512.0f);
  float m2 = 0.f;
#pragma unroll
  for (int i = 0; i < 4; ++i) { const float d = __uint_as_float(pv[i].x) * (1.0f / 32.0f) - mean; m2 += __uint_as_float(pv[i].y) + 32.0f * d * d; }
  m2 = quad_sum(m2);
  var = m2 * (1.0f / 512.0f);
}

DI void publish_x(rsrc_t rws, const float (&v)[8], unsigned xoff, unsigned stoff, int lane, u32x4& packed) {
  packed.x = cvtpk(v[0], v[1]); packed.y = cvtpk(v[2], v[3]); packed.z = cvtpk(v[4], v[5]); packed.w = cvtpk(v[6], v[7]);
  st128(packed, rws, (unsigned)lane * 16u, xoff);
  float r[8] = {bflo(packed.x), bfhi(packed.x), bflo(packed.y), bfhi(packed.y), bflo(packed.z), bfhi(packed.z), bflo(packed.w), bfhi(packed.w)};
  float s = 0.f;
#pragma unroll
  for (int i = 0; i < 8; ++i) s += r[i];
  s = quad_sum(s);
  const float mu = s * (1.0f / 32.0f);
  float m2 = 0.f;
#pragma unroll
  for (int i = 0; i < 8; ++i) { const float d = r[i] - mu; m2 += d * d; }
  m2 = quad_sum(m2);
  if (lane < 16) { u32x2 o = {__float_as_uint(s), __float_as_uint(m2)}; st64(o, rws, (unsigned)lane * 8u, stoff); }
}

template <bool FULL, class HOOK>
DI u32x4 attn_wave(const LAS char* lds, int hd, int qi, int jmin, int lane, HOOK hook) {
  const int ql = lane & 31, h = lane >> 5;
  const bf16x8 Q = ldsfrag(lds + A_Q + (hd * 2 + h) * 2048 + (32 * qi + ql) * 16);
  const LAS char* kb = lds + A_K + (hd * 2 + h) * PLN + (32 * qi + ql) * 16;
  const int G = lane >> 4, q4 = (lane & 15) >> 2, pp = lane & 3;
  const LAS char* vb = lds + ((G & 1) ? A_ONE : A_V + (hd * 2 + (pp >> 1)) * PLN) + (32 * qi + 4 * h + q4) * 16 + (pp & 1) * 8;
  constexpr float NEG = -1e30f;
  const f32x16 zero16 = {0.f,0.f,0.f,0.f,0.f,0.f,0.f,0.f,0.f,0.f,0.f,0.f,0.f,0.f,0.f,0.f};
  f32x16 biasD;
  {
    const LAS f32x4* bt = (const LAS f32x4*)(lds + T_MASK + lane * 16);
#pragma unroll
    for (int q = 0; q < 4; ++q) { const f32x4 v = bt[q * 64]; biasD[4 * q] = v[0]; biasD[4 * q + 1] = v[1]; biasD[4 * q + 2] = v[2]; biasD[4 * q + 3] = v[3]; }
  }
  float m0 = NEG, m1 = NEG;
  {
    bf16x8 ka = ldsfrag(kb + 8 * 512), kn = ka;
    if (FULL || 7 >= jmin) kn = ldsfrag(kb + 7 * 512);
    f32x16 sa = mfma32(ka, Q, biasD), sb = sa;
    SBAR();
#pragma unroll
    for (int j = 8; j >= 0; --j) {
      f32x16& cur = (j & 1) ? sb : sa; f32x16& nxt = (j & 1) ? sa : sb;
      if (j >= 1 && (FULL || j - 1 >= jmin)) {
        const bf16x8 kj = kn;
        if (j >= 2 && (FULL || j - 2 >= jmin)) kn = ldsfrag(kb + (j - 2) * 512);
        if (j - 1 == 0) { f32x16 bf;
#pragma unroll
          for (int r = 0; r < 16; ++r) bf[r] = NEG - biasD[r];
          nxt = mfma32(kj, Q, bf); }
        else nxt = mfma32(kj, Q, zero16);
      }
      if (FULL || j >= jmin) {
#pragma unroll
        for (int r = 0; r < 16; r += 4) { m0 = max3f(m0, cur[r], cur[r + 1]); m1 = max3f(m1, cur[r + 2], cur[r + 3]); }
        asm volatile("" : "+v"(m0), "+v"(m1));
      }
      SBAR();
    }
  }
  const float m = swap32_max(fmaxf(m0, m1));
  f32x16 negm;
#pragma unroll
  for (int r = 0; r < 16; ++r) negm[r] = -m;
  f32x16 o = zero16;
  {
    bf16x8 ka = ldsfrag(kb + 8 * 512), kn = ka;
    if (FULL || 7 >= jmin) kn = ldsfrag(kb + 7 * 512);
    f32x16 sa = mfma32(ka, Q, biasD + negm), sb = sa;
    SBAR();
#pragma unroll
    for (int j = 8; j >= 0; --j) {
      f32x16& cur = (j & 1) ? sb : sa; f32x16& nxt = (j & 1) ? sa : sb;
      const bool have = FULL || j >= jmin;
      hook(j);
      s16x4 v0 = {}, v1 = {}, v2 = {}, v3 = {};
      if (have) { v0 = trd(vb + (32 * j) * 16); v1 = trd(vb + (32 * j + 8) * 16); v2 = trd(vb + (32 * j + 16) * 16); v3 = trd(vb + (32 * j + 24) * 16); }
      if (j >= 1 && (FULL || j - 1 >= jmin)) {
        const bf16x8 kj = kn;
        if (j >= 2 && (FULL || j - 2 >= jmin)) kn = ldsfrag(kb + (j - 2) * 512);
        if (j - 1 == 0) { f32x16 cf;
#pragma unroll
          for (int r = 0; r < 16; ++r) cf[r] = (NEG - biasD[r]) - m;
          nxt = mfma32(kj, Q, cf); }
        else nxt = mfma32(kj, Q, negm);
      }
      if (have) {
        unsigned pk[8];
#pragma unroll
        for (int r = 0; r < 8; ++r) pk[r] = cvtpk(__builtin_amdgcn_exp2f(cur[2 * r]), __builtin_amdgcn_exp2f(cur[2 * r + 1]));
        o = mfma32(__builtin_shufflevector(v0, v1, 0, 1, 2, 3, 4, 5, 6, 7), asfrag(u32x4{pk[0], pk[1], pk[2], pk[3]}), o);
        o = mfma32(__builtin_shufflevector(v2, v3, 0, 1, 2, 3, 4, 5, 6, 7), asfrag(u32x4{pk[4], pk[5], pk[6], pk[7]}), o);
      }
      SBAR();
    }
  }
  const float inv = __builtin_amdgcn_rcpf(o[8]);
  return u32x4{cvtpk(o[0] * inv, o[1] * inv), cvtpk(o[2] * inv, o[3] * inv), cvtpk(o[4] * inv, o[5] * inv), cvtpk(o[6] * inv, o[7] * inv)};
}

__global__ __launch_bounds__(512, 2) void mega(Params P) {
  __shared__ __attribute__((aligned(16))) char lds_[LDS_BYTES];
  const LAS char* lds = (const LAS char*)lds_;
  LAS char* ldsw = (LAS char*)lds_;
  const unsigned ldsb = (unsigned)(size_t)lds_;
  int w = rfl(threadIdx.x >> 6);
  int lane = (int)__builtin_amdgcn_mbcnt_hi(~0u, __builtin_amdgcn_mbcnt_lo(~0u, 0u));
  const int b = blockIdx.x;
  int t = 4 * ((b & 7) >> 1) + (b >> 6), p = 8 * (b & 1) + ((b >> 3) & 7);
  int hh = p >> 3, cg = p & 7;
  int tt = w & 3, kh = w >> 2;
  int tauA = 8 * t + w, tauB = 8 * t + 4 * hh + tt, ksB = 2 * cg + kh, u32i = 2 * t + hh;
  int tok = lane & 15, g = lane >> 4;
  const rsrc_t rws = __builtin_amdgcn_make_buffer_rsrc(P.ws, 0, (int)WS_END, 0x00020000);
  bool dead = false;
  STAMP(0); RT(59);
  if (w == 7) {
    u64 pc;
    asm volatile("s_getpc_b64 %0" : "=s"(pc));
    const rsrc_t rc = __builtin_amdgcn_make_buffer_rsrc((void*)pc, 0, CODE_BYTES, 0x00020000);
    touch8k(rc, (unsigned)lane * 128u, (unsigned)((b >> 3) % 5) * 8192u, ldsb + T_DUMP);
    touch8k(rws, (unsigned)lane << 20, 0u, ldsb + T_DUMP);
  }
  u64 tag = 0;
  u32x4 resid = {0u, 0u, 0u, 0u};
  auto nohook = [](int) INL {};
  auto noload = [](int) INL { return bf16x8{}; };

  int nL = 2;
  asm volatile("" : "+s"(nL));
#pragma unroll 1
  for (int L = 0; L < nL; ++L) {
    asm volatile("" : "+s"(L));
    const unsigned v16 = (unsigned)lane * 16u;
    auto load_wq = [&](int w0, int nw) INL {
      if (w >= w0) for (int sl = w - w0; sl < 16; sl += nw) { const unsigned src = WS_WQT + (unsigned)((p * 16 + (sl ^ (2 * (p & 7)))) * 6) * 1024u, dst = ldsb + MAIN + (unsigned)sl * 6144u;
        dma4k(rws, (unsigned)lane * 16u, src, dst); dma2k(rws, (unsigned)lane * 16u, src + 4096u, dst + 4096u); }
    };
    auto load_w1 = [&](int h0, int h1, int w0, int nw) INL {
      if (p < NP1 && w >= w0) for (int hs = h0 + w - w0; hs < h1; hs += nw) { const int sl = hs >> 1;
        dma4k(rws, (unsigned)lane * 16u, WS_W1T + (unsigned)((p * 16 + (sl ^ (2 * (p & 7)))) * 8 + (hs & 1) * 4) * 1024u, ldsb + L_W1 + (unsigned)hs * 4096u); }
    };
    auto w2_half = [&](int i, int e) INL {
      const int slot = i >= 19 ? i - 19 : i, k = i ^ (p & 3);
      dma4k(rws, (unsigned)lane * 16u, WS_W2T + (unsigned)((cg * 56 + e * 28 + k) * 4) * 1024u, ldsb + MAIN + (unsigned)(slot * 8 + e * 4) * 1024u);
    };
    auto load_w2 = [&](int i0, int i1, int w0, int nw) INL {
      if (w >= w0) for (int q = 2 * i0 + w - w0; q < 2 * i1; q += nw) w2_half(q >> 1, q & 1);
    };
    (void)v16;
    FRESH();
    {
      bf16x8 X[16];
      float mean = 0.f, var = 0.f;
      const int rotA = 2 * (p & 7);
      f32x4 akv[4];
#pragma unroll
      for (int c = 0; c < 4; ++c) akv[c] = f32x4{0.f, 0.f, 0.f, 0.f};
      if (L == 0) {
        const rsrc_t rx = __builtin_amdgcn_make_buffer_rsrc((void*)P.x, 0, S_ * D_ * 2, 0x00020000);
        if (w < 2) {
          const rsrc_t rq = __builtin_amdgcn_make_buffer_rsrc((void*)P.wqkv, 0, D_ * 3 * D_ * 2, 0x00020000);
#pragma unroll
          for (int jj = 0; jj < 3; ++jj) { const int j = 3 * w + jj; dma16(rq, raw_voff<3>(j, lane, 3072u, (unsigned)p * 64u, 1024u), rfl((unsigned)t * 32u * 3072u), rfl(ldsb + L_PRE + 18 * KB + (unsigned)j * 1024u)); }
        } else if (w < 4) {
          if (p < NP1) {
            const rsrc_t r1 = __builtin_amdgcn_make_buffer_rsrc((void*)P.w1, 0, D_ * F_ * 2, 0x00020000);
#pragma unroll
            for (int jj = 0; jj < 4; ++jj) { const int j = 4 * (w - 2) + jj; dma16(r1, raw_voff<4>(j, lane, 3584u, (unsigned)p * 256u, 64u), rfl((unsigned)t * 32u * 3584u), rfl(ldsb + L_PRE + (unsigned)j * 1024u)); }
          }
        } else if (w == 4) {
          const rsrc_t ro = __builtin_amdgcn_make_buffer_rsrc((void*)P.wo, 0, D_ * D_ * 2, 0x00020000);
#pragma unroll
          for (int j = 0; j < 2; ++j) dma16(ro, raw_voff<1>(j, lane, 1024u, (unsigned)(cg * 128 + (u32i & 1) * 64), 0u), rfl((unsigned)(u32i >> 1) * 32u * 1024u), rfl(ldsb + L_PRE + 8 * KB + (unsigned)j * 1024u));
        } else if (w == 5 || (w == 6 && u32i < 24)) {
          const rsrc_t r2 = __builtin_amdgcn_make_buffer_rsrc((void*)P.w2, 0, F_ * D_ * 2, 0x00020000);
          const int ks = u32i + (w - 5) * 32;
#pragma unroll
          for (int j = 0; j < 4; ++j) dma16(r2, raw_voff<2>(j, lane, 1024u, (unsigned)cg * 128u, 64u), rfl((unsigned)ks * 32u * 1024u), rfl(ldsb + L_PRE + (10 + (w - 5) * 4) * KB + (unsigned)j * 1024u));
        }
#pragma unroll
        for (int r = 0; r < 16; ++r) { const int rr = r ^ (2 * (p & 7)); dma16(rx, (unsigned)lane * 16u, (unsigned)(16 * tauA + rr) * 1024u, ldsb + MAIN + (unsigned)((16 * w + rr) * XROW)); }
        u32x2 epoch_raw = ld64(rws, WS_EPOCH, 0);
        STAMP(23);
        VMWAIT0();
        tag = ((((u64)rfl(epoch_raw.y)) << 32) | rfl(epoch_raw.x)) + 1ull;
        BAR();
        STAMP(1);
        if (w < 6) st128(__builtin_bit_cast(u32x4, raw_frag<3, 0>(lds + L_PRE + 18 * KB, w, lane)), rws, (unsigned)lane * 16u, rfl(WS_WQT + (unsigned)((p * 16 + t) * 6 + w) * 1024u));
        VMWAIT0();
        BAR();
        if (w == 0 && lane == 0) set_flag(rws, FL_WT + (unsigned)(t * 16 + p) * 128u, tag);
        STAMP(2);
        FRESH_V();
        if (p < NP1) st128(__builtin_bit_cast(u32x4, raw_frag<4, 0>(lds + L_PRE, w, lane)), rws, (unsigned)lane * 16u, rfl(WS_W1T + (unsigned)((p * 16 + t) * 8 + w) * 1024u));
        if (w >= 6) st128(__builtin_bit_cast(u32x4, raw_frag<1, 1>(lds + L_PRE + 8 * KB, w - 6, lane)), rws, (unsigned)lane * 16u, rfl(WS_WOT + (unsigned)((cg * 16 + (u32i >> 1)) * 4 + (u32i & 1) * 2 + (w - 6)) * 1024u));
        if (w < 4 || u32i < 24) { const int hs = w >> 2, ks = u32i + hs * 32;
          st128(__builtin_bit_cast(u32x4, raw_frag<2, 0>(lds + L_PRE + (10 + hs * 4) * KB, w & 3, lane)), rws, (unsigned)lane * 16u, rfl(WS_W2T + (unsigned)((cg * 56 + ks) * 4 + (w & 3)) * 1024u)); }
        {
          const LAS char* xp = lds + MAIN + (16 * w + tok) * XROW + g * 8;
#pragma unroll
          for (int s = 0; s < 16; ++s) { const int ks = s ^ rotA; const u32x2 lo = *(const LAS u32x2*)(xp + ks * 64), hi = *(const LAS u32x2*)(xp + ks * 64 + 32); X[s] = asfrag(u32x4{lo.x, lo.y, hi.x, hi.y}); }
          f32x4 s1 = {0.f, 0.f, 0.f, 0.f}, s2 = {0.f, 0.f, 0.f, 0.f};
          const bf16x8 on = ones_frag();
#pragma unroll
          for (int i = 0; i < 16; ++i) { s1 = mfma16(on, X[i], s1); s2 = mfma16(X[i], X[i], s2); }
          mean = s1[0] * (1.0f / 512.0f);
          const int dj = tok - 4 * g;
          const float dv = dj == 0 ? s2[0] : dj == 1 ? s2[1] : dj == 2 ? s2[2] : dj == 3 ? s2[3] : 0.f;
          var = fmaxf(quad_sum(dv) * (1.0f / 512.0f) - mean * mean, 0.f);
        }
        if (w == 0) poll_flags(rws, FL_WT + (unsigned)((lane * 16 + p) * 128), 16, tag, lane, dead);
        BAR();
        STAMP(24);
        load_wq(0, 8);
        unsigned biasv = 0;
        if (w < 2) { if (p < NP1) biasv = P.b1[p * 128 + w * 64 + lane]; } else if (w == 2) biasv = P.b2[cg * 64 + lane];
        {
          const GAS u32x2* rp = (const GAS u32x2*)(P.x + (size_t)(16 * tauB + tok) * D_ + 32 * ksB + 4 * g);
          const u32x2 r0 = rp[0], r1 = rp[4];
          resid = u32x4{r0.x, r0.y, r1.x, r1.y};
        }
        if (w < 3) *(LAS float*)(ldsw + (w < 2 ? T_B1 + (w * 64 + lane) * 4 : T_B2 + lane * 4)) = bf2f(biasv);
        VMWAIT0();
        BAR();
        STAMP(27);
        if (w < 6) {
          f32x4 cs = {0.f, 0.f, 0.f, 0.f};
#pragma unroll
          for (int s = 0; s < 16; ++s) cs = mfma16(ldsfrag(lds + MAIN + (s * 6 + w) * KB + lane * 16), ones_frag(), cs);
          if (tok == 0) *(LAS f32x4*)(ldsw + T_CSQ + (w * 16 + 4 * g) * 4) = cs;
        }
        STAMP(16 + 32 * L);
        gemm_lds<4, 0, 16, 16, 0, false>(akv, X, lds + MAIN + 2 * KB + lane * 16, noload, [](int s) INL { return s * 6 * KB; }, nohook, rws, dead);
        BAR();
      } else {
        STAMP(16 + 32 * L);
        u32x2 pv[4];
        stats_load(rws, WS_ST2 + (unsigned)tauA * 2048u, lane, pv);
        const unsigned xb = WS_X2 + (unsigned)(tauA * 16 + rotA) * 1024u;
        auto xl = [&](int s) INL { return asfrag(ld128(rws, (unsigned)lane * 16u, xb ^ ((unsigned)s << 10))); };
#pragma unroll
        for (int s = 0; s < 8; ++s) X[s] = xl(s);
        stats_from_partials(pv, mean, var);
        gemm_lds<4, 0, 16, 16, 8, false>(akv, X, lds + MAIN + 2 * KB + lane * 16, xl, [](int s) INL { return s * 6 * KB; }, nohook, rws, dead);
      }
      FRESH();
      const float rs = __builtin_amdgcn_rsqf(var + EPS), rm = rs * mean;
      auto qkv_pack = [&](const f32x4& a0, const f32x4& a1, int c0, float sc) INL {
        const f32x4 cs0 = *(const LAS f32x4*)(lds + T_CSQ + (c0 * 16 + 4 * g) * 4), cs1 = *(const LAS f32x4*)(lds + T_CSQ + (c0 * 16 + 16 + 4 * g) * 4);
        float v[8];
#pragma unroll
        for (int j = 0; j < 4; ++j) { v[j] = (rs * a0[j] - rm * cs0[j]) * sc; v[4 + j] = (rs * a1[j] - rm * cs1[j]) * sc; }
        auto s0 = __builtin_amdgcn_permlane16_swap(cvtpk(v[0], v[1]), cvtpk(v[4], v[5]), false, false);
        auto s1 = __builtin_amdgcn_permlane16_swap(cvtpk(v[2], v[3]), cvtpk(v[6], v[7]), false, false);
        return u32x4{s0[0], s1[0], s0[1], s1[1]};
      };
      {
        const u32x4 ko = qkv_pack(akv[0], akv[1], 2, 1.0f), vo = qkv_pack(akv[2], akv[3], 4, 1.0f);
        st128(ko, rws, (unsigned)(PLANE * 2048 + TL * 16), WS_KG + (unsigned)((L * 16 + t) * 16 + p) * 8192u);
        st128(vo, rws, (unsigned)(PLANE * 2048 + TL * 16), WS_VG + (unsigned)((L * 16 + t) * 16 + p) * 8192u);
        *(LAS u32x4*)(ldsw + A_K + PLANE * PLN + (256 + TL) * 16) = ko;
        *(LAS u32x4*)(ldsw + A_V + PLANE * PLN + (256 + TL) * 16) = vo;
      }
      STAMP(3 + 32 * L);
      FRESH();
      f32x4 aq[2] = {f32x4{0.f, 0.f, 0.f, 0.f}, f32x4{0.f, 0.f, 0.f, 0.f}};
      gemm_lds<2, 0, 8, 16, 0, false>(aq, X, lds + MAIN + lane * 16, noload, [](int s) INL { return s * 6 * KB; }, nohook, rws, dead);
      VMWAIT0();
      BAR();
      if (w == 0 && lane == 0) set_flag(rws, flag_row(0, K_KV) + (unsigned)(L * 5 * 256 + t * 16 + p) * 128u, tag);
      gemm_lds<2, 8, 16, 16, 0, false>(aq, X, lds + MAIN + lane * 16, noload, [](int s) INL { return s * 6 * KB; }, nohook, rws, dead);
      STAMP(4 + 32 * L);
      FRESH();
      const u32x4 qo = qkv_pack(aq[0], aq[1], 0, QSCALE);
      if (w == 0) {
        if (L == 0) {
          const int grp = lane >> 4, pg = grp == 0 ? p : cg + 8 * (grp - 1);
          poll_flags(rws, flag_row(0, K_KV) + (unsigned)(((lane & 15) * 16 + pg) * 128), 48, tag, lane, dead);
        } else poll_flags(rws, flag_row(1, K_KV) + (unsigned)(((t - 1 - lane) * 16 + p) * 128), t < 2 ? t : 2, tag, lane, dead);
      }
      BAR();
      STAMP(5 + 32 * L);
      FRESH();
      {
        const int d = 1 + (w >> 2);
        u32x4 hv[4] = {u32x4{}, u32x4{}, u32x4{}, u32x4{}};
        if (t - d >= 0) {
          const unsigned hb = ((w & 2) ? WS_VG : WS_KG) + (unsigned)((L * 16 + t - d) * 16 + p) * 8192u + (unsigned)(w & 1) * 4096u;
#pragma unroll
          for (int i = 0; i < 4; ++i) hv[i] = ld128(rws, (unsigned)lane * 16u, hb + (unsigned)i * 1024u);
        }
        *(LAS u32x4*)(ldsw + A_Q + PLANE * 2048 + TL * 16) = qo;
        if (w * 64 + lane < 400) *(LAS u32x4*)(ldsw + A_ONE + (w * 64 + lane) * 16) = __builtin_bit_cast(u32x4, ones_frag());
        { const int r0 = 4 * (w & 3) + 2 * (w >> 2), key = (r0 & 3) + 8 * (r0 >> 2), dq = (lane & 31) - 4 * (lane >> 5);
          *(LAS f32x2*)(ldsw + T_MASK + (w & 3) * 1024 + lane * 16 + (w >> 2) * 8) = f32x2{key <= dq ? 0.f : -1e30f, key + 1 <= dq ? 0.f : -1e30f}; }
        if (t - d >= 0) {
#pragma unroll
          for (int i = 0; i < 4; ++i) *(LAS u32x4*)(ldsw + ((w & 2) ? A_V : A_K) + (2 * (w & 1) + (i >> 1)) * PLN + ((2 - d) * 128 + (i & 1) * 64 + lane) * 16) = hv[i];
        }
      }
      BAR();
      STAMP(6 + 32 * L);
    }
    FRESH();
    {
      if (w >= 4 && p < NP1) touch8k(rws, (unsigned)lane * 128u, WS_W1T + (unsigned)(p * 128 + (t & 3) * 32 + (w - 4) * 8) * 1024u, ldsb + T_DUMP);
      if (w == 2 || w == 3) touch8k(rws, (unsigned)lane * 128u, WS_WOT + (unsigned)(cg * 64 + (t & 3) * 16 + (w - 2) * 8) * 1024u, ldsb + T_DUMP);
      STAMP(17 + 32 * L);
      u32x4 ov;
      if (t >= 2) ov = attn_wave<true>(lds, w & 1, w >> 1, 0, lane, nohook); else ov = attn_wave<false>(lds, w & 1, w >> 1, 8 - 4 * t - (w >> 1), lane, nohook);
      FRESH();
      st128(ov, rws, (unsigned)(((lane & 31) >> 4) * 16384 + ((2 * (w & 1) + (lane >> 5)) * 16 + (lane & 15)) * 16), WS_OG + (unsigned)((L * 128 + 8 * t + 2 * (w >> 1)) * 16 + p) * 1024u);
      STAMP(18 + 32 * L);
      if (w < 4) {
#pragma unroll
        for (int i = 0; i < 4; ++i) { const int sl = w + 4 * i; dma4k(rws, (unsigned)lane * 16u, WS_WOT + (unsigned)((cg * 16 + (sl ^ cg)) * 4) * 1024u, ldsb + L_WO + (unsigned)sl * 4096u); }
      }
      VMWAIT0();
      BAR();
      if (w == 0 && lane == 0) set_flag(rws, flag_row(0, K_O) + (unsigned)(L * 5 * 256 + t * 16 + p) * 128u, tag);
      STAMP(7 + 32 * L);
      load_w1(14, 32, 1, 7);
      if (w == 0) poll_flags(rws, flag_row(0, K_O) + (unsigned)(L * 5 * 256 + t * 16 + lane) * 128u, 16, tag, lane, dead);
      BAR();
      STAMP(8 + 32 * L);
    }
    FRESH();
    {
      bf16x8 Xo[8];
      const unsigned xb = WS_OG + (unsigned)((L * 128 + tauB) * 16 + 8 * kh + cg) * 1024u;
      auto xl = [&](int i) INL { return asfrag(ld128(rws, (unsigned)lane * 16u, xb ^ ((unsigned)i << 10))); };
#pragma unroll
      for (int i = 0; i < 8; ++i) Xo[i] = xl(i);
      f32x4 acc[4];
#pragma unroll
      for (int c = 0; c < 4; ++c) acc[c] = f32x4{0.f, 0.f, 0.f, 0.f};
      gemm_lds<4, 0, 8, 8, 0, false>(acc, Xo, lds + L_WO + kh * 32 * KB + lane * 16, xl, [](int s) INL { return s * 4 * KB; }, nohook, rws, dead);
      STAMP(19 + 32 * L);
      FRESH();
      *(LAS f32x4*)(ldsw + MAIN + (w * 2 + 0) * KB + lane * 16) = kh ? acc[0] : acc[2];
      *(LAS f32x4*)(ldsw + MAIN + (w * 2 + 1) * KB + lane * 16) = kh ? acc[1] : acc[3];
      BAR();
      const f32x4 o0 = *(const LAS f32x4*)(lds + MAIN + ((w ^ 4) * 2 + 0) * KB + lane * 16), o1 = *(const LAS f32x4*)(lds + MAIN + ((w ^ 4) * 2 + 1) * KB + lane * 16);
      const f32x4 k0 = kh ? acc[2] : acc[0], k1 = kh ? acc[3] : acc[1];
      float v[8];
      v[0] = k0[0] + o0[0] + bflo(resid.x); v[1] = k0[1] + o0[1] + bfhi(resid.x); v[2] = k0[2] + o0[2] + bflo(resid.y); v[3] = k0[3] + o0[3] + bfhi(resid.y);
      v[4] = k1[0] + o1[0] + bflo(resid.z); v[5] = k1[1] + o1[1] + bfhi(resid.z); v[6] = k1[2] + o1[2] + bflo(resid.w); v[7] = k1[3] + o1[3] + bfhi(resid.w);
      publish_x(rws, v, WS_X1 + (unsigned)((L * 128 + tauB) * 16 + ksB) * 1024u, WS_ST1 + (unsigned)((L * 128 + tauB) * 16 + ksB) * 128u, lane, resid);
      VMWAIT0();
      BAR();
      if (w == 0 && lane == 0) set_flag(rws, flag_row(0, K_X1) + (unsigned)(L * 5 * 256 + t * 16 + p) * 128u, tag);
      STAMP(9 + 32 * L);
      load_w1(0, 14, 1, 7);
      if (w >= 1) touch8k(rws, (unsigned)lane * 128u, WS_W2T + (unsigned)(cg * 224 + (t & 3) * 56 + (w - 1) * 8) * 1024u, ldsb + T_DUMP);
      if (w == 0) poll_flags(rws, flag_row(0, K_X1) + (unsigned)(L * 5 * 256 + t * 16 + lane) * 128u, 16, tag, lane, dead);
      if (w >= 1) asm volatile("s_waitcnt vmcnt(1)" ::: "memory"); else VMWAIT0();
      BAR();
      STAMP(10 + 32 * L);
    }
    FRESH();
    if (p < NP1) {
      bf16x8 X1[16];
      u32x2 pv1[4];
      stats_load(rws, WS_ST1 + (unsigned)(L * 128 + tauA) * 2048u, lane, pv1);
      const unsigned xb = WS_X1 + (unsigned)((L * 128 + tauA) * 16 + 2 * (p & 7)) * 1024u;
      auto xl = [&](int s) INL { return asfrag(ld128(rws, (unsigned)lane * 16u, xb ^ ((unsigned)s << 10))); };
#pragma unroll
      for (int s = 0; s < 8; ++s) X1[s] = xl(s);
      if (L == 0) {
        f32x4 cs = {0.f, 0.f, 0.f, 0.f};
#pragma unroll
        for (int s = 0; s < 16; ++s) cs = mfma16(ldsfrag(lds + L_W1 + (s * 8 + w) * KB + lane * 16), ones_frag(), cs);
        if (tok == 0) *(LAS f32x4*)(ldsw + T_CS1 + (w * 16 + 4 * g) * 4) = cs;
      }
      float mean1, var1;
      stats_from_partials(pv1, mean1, var1);
      STAMP(26 + 32 * L);
      f32x4 acc[8];
#pragma unroll
      for (int c = 0; c < 8; ++c) acc[c] = f32x4{0.f, 0.f, 0.f, 0.f};
      auto hook = [&](int s) INL {
        if (s == 13 && w < 6) w2_half(w >> 1, w & 1);
        if (s == 14 && L == 0 && w < 3) touch8k(rws, (unsigned)lane * 128u, WS_WQT + (unsigned)(p * 96 + (t & 3) * 24 + w * 8) * 1024u, ldsb + T_DUMP);
      };
      gemm_lds<8, 0, 16, 16, 8, false>(acc, X1, lds + L_W1 + lane * 16, xl, [](int s) INL { return s * 8 * KB; }, hook, rws, dead);
      BAR();
      STAMP(11 + 32 * L);
      FRESH_V();
      const float r1 = __builtin_amdgcn_rsqf(var1 + EPS), r2 = __builtin_amdgcn_rsqf(var1 * r1 * r1 + EPS), rs = r1 * r2, rm = rs * mean1;
      constexpr float C1 = -2.0f * 0.7978845608028654f * 1.4426950408889634f, C2 = C1 * 0.044715f;
      const f32x2 rs2 = {rs, rs}, nrm2 = {-rm, -rm}, c1v = {C1, C1}, c2v = {C2, C2}, one2 = {1.0f, 1.0f};
#pragma unroll
      for (int u = 0; u < 4; ++u) {
        f32x2 av[4], e[4];
#pragma unroll
        for (int hf = 0; hf < 2; ++hf) {
          const int c = 2 * u + hf;
          const f32x4 cs = *(const LAS f32x4*)(lds + T_CS1 + (c * 16 + 4 * g) * 4), bb = *(const LAS f32x4*)(lds + T_B1 + (c * 16 + 4 * g) * 4);
          av[2 * hf] = rs2 * f32x2{acc[c][0], acc[c][1]} + (nrm2 * f32x2{cs[0], cs[1]} + f32x2{bb[0], bb[1]});
          av[2 * hf + 1] = rs2 * f32x2{acc[c][2], acc[c][3]} + (nrm2 * f32x2{cs[2], cs[3]} + f32x2{bb[2], bb[3]});
        }
#pragma unroll
        for (int i = 0; i < 4; ++i) e[i] = av[i] * (c2v * (av[i] * av[i]) + c1v);
#pragma unroll
        for (int i = 0; i < 4; ++i) e[i] = f32x2{__builtin_amdgcn_exp2f(e[i].x), __builtin_amdgcn_exp2f(e[i].y)} + one2;
#pragma unroll
        for (int i = 0; i < 4; ++i) e[i] = av[i] * f32x2{__builtin_amdgcn_rcpf(e[i].x), __builtin_amdgcn_rcpf(e[i].y)};
        st128(u32x4{cvtpk2(e[0]), cvtpk2(e[1]), cvtpk2(e[2]), cvtpk2(e[3])}, rws, (unsigned)lane * 16u, WS_HG + (unsigned)((L * 128 + tauA) * 56 + 4 * p + u) * 1024u);
        SBAR();
        w2_half(3 + 4 * u + (w >> 1), w & 1);
        SBAR();
      }
      STAMP(20 + 32 * L);
      asm volatile("s_waitcnt vmcnt(4)" ::: "memory");
      BAR();
      if (w == 0 && lane == 0) set_flag(rws, flag_row(0, K_H) + (unsigned)(L * 5 * 256 + t * 16 + p) * 128u, tag);
    } else {
      if (L == 1 && b == 255 && w == 0) {
        bool d2 = dead;
#pragma unroll 1
        for (int i = 0; i < 4; ++i) poll_flags(rws, FL_WT + (unsigned)(i * 64 + lane) * 128u, 64, tag, lane, d2);
        if (lane == 0) set_flag(rws, WS_EPOCH, tag);
      }
      load_w2(0, 19, 0, 8);
    }
    FRESH();
    {
      STAMP(12 + 32 * L);
      if (w == 0) poll_flags(rws, flag_row(0, K_H) + (unsigned)(L * 5 * 256 + t * 16 + lane) * 128u, NP1, tag, lane, dead);
      VMWAIT0();
      BAR();
      STAMP(13 + 32 * L);
      bf16x8 Xh[28];
      const unsigned xb = WS_HG + (unsigned)((L * 128 + tauB) * 56 + 28 * kh + (p & 3)) * 1024u;
      auto xl = [&](int i) INL { return asfrag(ld128(rws, (unsigned)lane * 16u, (xb ^ ((unsigned)(i & 3) << 10)) + (unsigned)(i & ~3) * 1024u)); };
#pragma unroll
      for (int i = 0; i < 10; ++i) Xh[i] = xl(i);
      f32x4 acc[4];
#pragma unroll
      for (int c = 0; c < 4; ++c) acc[c] = f32x4{0.f, 0.f, 0.f, 0.f};
      gemm_lds<4, 0, 9, 28, 10, false>(acc, Xh, lds + MAIN + kh * 4 * KB + lane * 16, xl, [](int s) INL { return s * 8 * KB; }, nohook, rws, dead);
      STAMP(21 + 32 * L);
      BAR();
      load_w2(19, 28, 0, 8);
      gemm_lds<4, 9, 19, 28, 10, false>(acc, Xh, lds + MAIN + kh * 4 * KB + lane * 16, xl, [](int s) INL { return s * 8 * KB; }, nohook, rws, dead);
      VMWAIT0();
      BAR();
      STAMP(22 + 32 * L);
      gemm_lds<4, 19, 28, 28, 10, false>(acc, Xh, lds + MAIN + kh * 4 * KB + lane * 16, xl, [](int s) INL { return (s - 19) * 8 * KB; }, nohook, rws, dead);
      STAMP(14 + 32 * L);
      FRESH();
      *(LAS f32x4*)(ldsw + L_RED2 + (w * 2 + 0) * KB + lane * 16) = kh ? acc[0] : acc[2];
      *(LAS f32x4*)(ldsw + L_RED2 + (w * 2 + 1) * KB + lane * 16) = kh ? acc[1] : acc[3];
      BAR();
      const f32x4 o0 = *(const LAS f32x4*)(lds + L_RED2 + ((w ^ 4) * 2 + 0) * KB + lane * 16), o1 = *(const LAS f32x4*)(lds + L_RED2 + ((w ^ 4) * 2 + 1) * KB + lane * 16);
      const f32x4 k0 = kh ? acc[2] : acc[0], k1 = kh ? acc[3] : acc[1];
      const f32x4 b0 = *(const LAS f32x4*)(lds + T_B2 + (kh * 32 + 4 * g) * 4), b1v = *(const LAS f32x4*)(lds + T_B2 + (kh * 32 + 16 + 4 * g) * 4);
      float v[8];
      v[0] = k0[0] + o0[0] + b0[0] + bflo(resid.x); v[1] = k0[1] + o0[1] + b0[1] + bfhi(resid.x); v[2] = k0[2] + o0[2] + b0[2] + bflo(resid.y); v[3] = k0[3] + o0[3] + b0[3] + bfhi(resid.y);
      v[4] = k1[0] + o1[0] + b1v[0] + bflo(resid.z); v[5] = k1[1] + o1[1] + b1v[1] + bfhi(resid.z); v[6] = k1[2] + o1[2] + b1v[2] + bflo(resid.w); v[7] = k1[3] + o1[3] + b1v[3] + bfhi(resid.w);
      if (L == 1) {
        const unsigned a0 = cvtpk(v[0], v[1]), a1 = cvtpk(v[2], v[3]), c0 = cvtpk(v[4], v[5]), c1 = cvtpk(v[6], v[7]);
        auto s0 = __builtin_amdgcn_permlane16_swap(a0, c0, false, false);
        auto s1 = __builtin_amdgcn_permlane16_swap(a1, c1, false, false);
        const u32x4 o = {s0[0], s1[0], s0[1], s1[1]};
        const int choff = (g & 1) * 16 + (g >> 1) * 8;
        { const rsrc_t ro = __builtin_amdgcn_make_buffer_rsrc((void*)P.out, 0, S_ * D_ * 2, 0x00020000);
          __builtin_amdgcn_raw_buffer_store_b128(o, ro, (unsigned)(((16 * tauB + tok) * D_ + 32 * ksB + choff) * 2), 0, OUT_AUX); }
        STAMP(15 + 32 * L); RT(60); STAMP_OUT();
        break;
      }
      publish_x(rws, v, WS_X2 + (unsigned)(tauB * 16 + ksB) * 1024u, WS_ST2 + (unsigned)(tauB * 16 + ksB) * 128u, lane, resid);
      VMWAIT0();
      BAR();
      if (w == 0 && lane == 0) set_flag(rws, flag_row(0, K_X2) + (unsigned)(t * 16 + p) * 128u, tag);
      load_wq(1, 7);
      if (w == 0) poll_flags(rws, flag_row(0, K_X2) + (unsigned)(t * 16 + lane) * 128u, 16, tag, lane, dead);
      VMWAIT0();
      BAR();
      STAMP(15 + 32 * L);
    }
  }
}

extern "C" void kernel_launch(void* const* d_in, const int* in_sizes, int n_in, void* d_out, int out_size, void* d_ws, size_t ws_size, hipStream_t stream) {
  Params P;
  P.x = (const u16*)d_in[0]; P.wqkv = (const u16*)d_in[1]; P.wo = (const u16*)d_in[2];
  P.w1 = (const u16*)d_in[4]; P.b1 = (const u16*)d_in[5]; P.w2 = (const u16*)d_in[6]; P.b2 = (const u16*)d_in[7];
  P.out = (u16*)d_out; P.ws = (unsigned char*)d_ws;
  mega<<<256, 512, 0, stream>>>(P);
}
```

```cpp
#include <hip/hip_runtime.h>
#include <stdint.h>

typedef unsigned short u16;
typedef unsigned long long u64;
typedef short bf16x8 __attribute__((ext_vector_type(8)));
typedef short s16x4 __attribute__((ext_vector_type(4)));
typedef float f32x2 __attribute__((ext_vector_type(2)));
typedef float f32x4 __attribute__((ext_vector_type(4)));
typedef float f32x16 __attribute__((ext_vector_type(16)));
typedef unsigned u32x4 __attribute__((ext_vector_type(4)));
typedef unsigned u32x2 __attribute__((ext_vector_type(2)));
typedef __bf16 bf16x2_t __attribute__((ext_vector_type(2)));
#define LAS __attribute__((address_space(3)))
#define GAS __attribute__((address_space(1)))
#define DI __device__ __forceinline__
#define INL __attribute__((always_inline))
typedef __amdgpu_buffer_rsrc_t rsrc_t;

constexpr int S_ = 2048, D_ = 512, F_ = 1792;
constexpr float EPS = 1e-5f;
constexpr float QSCALE = 0.25f * 1.4426950408889634f;
constexpr int NP1 = 14;

constexpr unsigned WS_EPOCH = 0, WS_ERR = 128, WS_FLAGS = 4096;
enum { K_KV = 0, K_O = 1, K_X1 = 2, K_H = 3, K_X2 = 4 };
constexpr unsigned flag_row(int L, int kind) { return WS_FLAGS + (unsigned)((L * 5 + kind) * 256 * 128); }
constexpr unsigned FL_WT = flag_row(1, K_X2);
constexpr unsigned WS_WQT = 0x60000;
constexpr unsigned WS_WOT = WS_WQT + 16 * 96 * 1024;
constexpr unsigned WS_W1T = WS_WOT + 8 * 64 * 1024;
constexpr unsigned WS_W2T = WS_W1T + NP1 * 128 * 1024;
constexpr unsigned WS_KG = WS_W2T + 8 * 224 * 1024;
constexpr unsigned WS_VG = WS_KG + 2 * 256 * 8192;
constexpr unsigned WS_OG = WS_VG + 2 * 256 * 8192;
constexpr unsigned WS_X1 = WS_OG + 2 * 128 * 16 * 1024;
constexpr unsigned WS_ST1 = WS_X1 + 2 * 128 * 16 * 1024;
constexpr unsigned WS_HG = WS_ST1 + 2 * 128 * 16 * 128;
constexpr unsigned WS_X2 = WS_HG + 2 * 128 * 56 * 1024;
constexpr unsigned WS_ST2 = WS_X2 + 128 * 16 * 1024;
constexpr unsigned WS_DBG = WS_ST2 + 128 * 16 * 128;
constexpr unsigned WS_END = WS_DBG + 256 * 8 * 128 * 8;

constexpr int KB = 1024;
constexpr int CODE_BYTES = 36 * 1024;
constexpr int LDS_BYTES = 163840;
constexpr int T_CSQ = 0;
constexpr int T_CS1 = 512;
constexpr int T_B1 = 1024;
constexpr int T_B2 = 1536;
constexpr int T_DUMP = 1792;
constexpr int MAIN = 2048;
constexpr int PLN = 384 * 16 + 64;
constexpr int A_K = MAIN + 96 * KB, A_V = A_K + 4 * PLN, A_Q = MAIN, A_ONE = A_Q + 4 * 2048;
static_assert(A_V + 4 * PLN <= LDS_BYTES && A_ONE + 6400 <= MAIN + 16 * KB, "attention buffers");
constexpr int T_MASK = MAIN + 148 * KB;
static_assert(A_V + 4 * PLN <= T_MASK && T_MASK + 4096 <= LDS_BYTES, "mask table");
constexpr int L_W1 = MAIN + 30 * KB;
constexpr int L_WO = MAIN + 16 * KB;
constexpr int L_PRE = MAIN + 132 * KB;
constexpr int XROW = 1040;
static_assert(128 * XROW <= 132 * KB && L_PRE + 24 * KB <= LDS_BYTES, "prologue scratch");
constexpr int L_RED2 = MAIN + 96 * KB;
static_assert(L_W1 + 128 * KB <= LDS_BYTES && L_WO + 64 * KB <= LDS_BYTES, "images");

template <int V> struct IC { static constexpr int value = V; };
struct Params {
  const u16 *x, *wqkv, *wo, *w1, *b1, *w2, *b2;
  u16* out;
  unsigned char* ws;
};

DI float bf2f(unsigned v) { return __uint_as_float(v << 16); }
DI float bflo(unsigned v) { return __uint_as_float(v << 16); }
DI float bfhi(unsigned v) { return __uint_as_float(v & 0xffff0000u); }
DI unsigned cvtpk(float lo, float hi) { f32x2 v = {lo, hi}; bf16x2_t b = __builtin_convertvector(v, bf16x2_t); return __builtin_bit_cast(unsigned, b); }
DI unsigned cvtpk2(f32x2 v) { bf16x2_t b = __builtin_convertvector(v, bf16x2_t); return __builtin_bit_cast(unsigned, b); }
DI f32x4 mfma16(bf16x8 a, bf16x8 b, f32x4 c) { return __builtin_amdgcn_mfma_f32_16x16x32_bf16(a, b, c, 0, 0, 0); }
DI f32x16 mfma32(bf16x8 a, bf16x8 b, f32x16 c) { return __builtin_amdgcn_mfma_f32_32x32x16_bf16(a, b, c, 0, 0, 0); }
DI bf16x8 ones_frag() { unsigned o = 0x3F803F80u; asm volatile("" : "+v"(o)); return __builtin_bit_cast(bf16x8, u32x4{o, o, o, o}); }
DI bf16x8 asfrag(u32x4 v) { return __builtin_bit_cast(bf16x8, v); }
DI s16x4 trd(const LAS char* p) { return __builtin_bit_cast(s16x4, __builtin_amdgcn_ds_read_tr16_b64_v4i16((LAS s16x4*)p)); }
DI bf16x8 ldsfrag(const LAS char* p) { return *(const LAS bf16x8*)p; }
DI float max3f(float a, float b, float c) { return __builtin_fmaxf(__builtin_fmaxf(a, b), c); }
DI float swap32_add(float x) { auto r = __builtin_amdgcn_permlane32_swap(__float_as_uint(x), __float_as_uint(x), false, false); return __uint_as_float(r[0]) + __uint_as_float(r[1]); }
DI float swap16_add(float x) { auto r = __builtin_amdgcn_permlane16_swap(__float_as_uint(x), __float_as_uint(x), false, false); return __uint_as_float(r[0]) + __uint_as_float(r[1]); }
DI float quad_sum(float x) { return swap32_add(swap16_add(x)); }
DI float swap32_max(float x) { auto r = __builtin_amdgcn_permlane32_swap(__float_as_uint(x), __float_as_uint(x), false, false); return fmaxf(__uint_as_float(r[0]), __uint_as_float(r[1])); }

#define FRESH() do { asm volatile("" : "+v"(lane), "+s"(w), "+s"(t), "+s"(p)); tok = lane & 15; g = lane >> 4; hh = p >> 3; cg = p & 7; tt = w & 3; kh = w >> 2; \
  tauA = 8 * t + w; tauB = 8 * t + 4 * hh + tt; ksB = 2 * cg + kh; u32i = 2 * t + hh; } while (0)
#define PLANE ((g & 1) * 2 + (g >> 1))
#define TL (16 * w + tok)
#define FRESH_V() do { asm volatile("" : "+v"(lane)); tok = lane & 15; g = lane >> 4; } while (0)
#define BAR() asm volatile("s_waitcnt lgkmcnt(0)\n\ts_barrier" ::: "memory")
#define VMWAIT0() asm volatile("s_waitcnt vmcnt(0)" ::: "memory")
#define SBAR() __builtin_amdgcn_sched_barrier(0)
constexpr int SC1 = 16;
#define PDA 4
#define PDB 6
#define OUT_AUX 16
#define XAUX 16
DI u32x4 ld128(rsrc_t r, unsigned voff, unsigned soff) { return __builtin_amdgcn_raw_buffer_load_b128(r, voff, soff, XAUX); }
DI u32x2 ld64(rsrc_t r, unsigned voff, unsigned soff) { return __builtin_amdgcn_raw_buffer_load_b64(r, voff, soff, SC1); }
DI void st128(u32x4 v, rsrc_t r, unsigned voff, unsigned soff) { __builtin_amdgcn_raw_buffer_store_b128(v, r, voff + soff, 0, SC1); }
DI void st64(u32x2 v, rsrc_t r, unsigned voff, unsigned soff) { __builtin_amdgcn_raw_buffer_store_b64(v, r, voff + soff, 0, SC1); }
DI unsigned rfl(unsigned x) { return __builtin_amdgcn_readfirstlane(x); }
DI void dma16(rsrc_t r, unsigned voff, unsigned soff, unsigned ldsaddr) {
  asm volatile("s_mov_b32 m0, %0\n\ts_nop 3\n\tbuffer_load_dwordx4 %1, %2, %3 offen lds" :: "s"(rfl(ldsaddr)), "v"(voff), "s"(r), "s"(rfl(soff)) : "memory");
}
DI void dma16_sc1(rsrc_t r, unsigned voff, unsigned soff, unsigned ldsaddr) {
  asm volatile("s_mov_b32 m0, %0\n\ts_nop 3\n\tbuffer_load_dwordx4 %1, %2, %3 offen sc1 lds" :: "s"(rfl(ldsaddr)), "v"(voff), "s"(r), "s"(rfl(soff)) : "memory");
}
DI void dma4k(rsrc_t r, unsigned voff, unsigned soff, unsigned ldsaddr) {
  asm volatile("s_mov_b32 m0, %0\n\ts_nop 3\n\tbuffer_load_dwordx4 %1, %2, %3 offen sc1 lds\n\tbuffer_load_dwordx4 %1, %2, %3 offen offset:1024 sc1 lds\n\t"
               "buffer_load_dwordx4 %1, %2, %3 offen offset:2048 sc1 lds\n\tbuffer_load_dwordx4 %1, %2, %3 offen offset:3072 sc1 lds" :: "s"(rfl(ldsaddr)), "v"(voff), "s"(r), "s"(rfl(soff)) : "memory");
}
DI void dma2k(rsrc_t r, unsigned voff, unsigned soff, unsigned ldsaddr) {
  asm volatile("s_mov_b32 m0, %0\n\ts_nop 3\n\tbuffer_load_dwordx4 %1, %2, %3 offen sc1 lds\n\tbuffer_load_dwordx4 %1, %2, %3 offen offset:1024 sc1 lds" :: "s"(rfl(ldsaddr)), "v"(voff), "s"(r), "s"(rfl(soff)) : "memory");
}
DI void touch8k(rsrc_t r, unsigned voff, unsigned soff, unsigned ldsdump) {
  asm volatile("s_mov_b32 m0, %0\n\ts_nop 3\n\tbuffer_load_dword %1, %2, %3 offen sc1 lds" :: "s"(rfl(ldsdump)), "v"(voff), "s"(r), "s"(rfl(soff)) : "memory");
}

#define POLL_GAP 3
constexpr unsigned SPIN_CAP = 1u << 14;
DI void poll_flags(rsrc_t rws, unsigned voff, int n, u64 tag, int lane, bool& dead) {
  if (dead) return;
  const unsigned vo = lane < n ? voff : WS_EPOCH + 64u;
  const bool idle = lane >= n;
  u32x2 v0, v1, v2;
#define POLL_ISSUE(v) asm volatile("buffer_load_dwordx2 %0, %1, %2, 0 offen sc1" : "=v"(v) : "v"(vo), "s"(rws) : "memory")
#define POLL_OK(v) __all(idle || (v.x == (unsigned)tag && v.y == (unsigned)(tag >> 32)))
  asm volatile("s_waitcnt vmcnt(0)" ::: "memory");
  POLL_ISSUE(v0); __builtin_amdgcn_s_sleep(POLL_GAP);
  POLL_ISSUE(v1); __builtin_amdgcn_s_sleep(POLL_GAP);
  POLL_ISSUE(v2);
  for (unsigned spins = 0;;) {
    asm volatile("s_waitcnt vmcnt(2)" : "+v"(v0) :: "memory");
    if (POLL_OK(v0)) break;
    POLL_ISSUE(v0);
    asm volatile("s_waitcnt vmcnt(2)" : "+v"(v1) :: "memory");
    if (POLL_OK(v1)) break;
    POLL_ISSUE(v1);
    asm volatile("s_waitcnt vmcnt(2)" : "+v"(v2) :: "memory");
    if (POLL_OK(v2)) break;
    POLL_ISSUE(v2);
    if (++spins > SPIN_CAP) { dead = true; break; }
  }
  asm volatile("s_waitcnt vmcnt(0)" : "+v"(v0), "+v"(v1), "+v"(v2) :: "memory");
  if (dead && lane == 0) __builtin_amdgcn_raw_buffer_store_b32(1u, rws, WS_ERR, 0, SC1);
#undef POLL_ISSUE
#undef POLL_OK
}
DI void set_flag(rsrc_t rws, unsigned off, u64 tag) { u32x2 v = {(unsigned)tag, (unsigned)(tag >> 32)}; st64(v, rws, off, 0); }

template <int NP> DI unsigned raw_voff(int j, int lane, unsigned rowpitch, unsigned cb0, unsigned cbs) {
  const int beta = 2 * j + (lane >> 5), oct = beta / NP, pair = beta % NP, r = (lane >> 2) & 7, q4 = lane & 3;
  return (unsigned)(8 * oct + r) * rowpitch + cb0 + (unsigned)pair * cbs + (unsigned)((((q4 >> 1) ^ (r >> 2) ^ oct) & 1) * 32 + (q4 & 1) * 16);
}
template <int NP, int ORD> DI bf16x8 raw_frag(const LAS char* kbase, int ct, int lane) {
  const int g = lane >> 4, q = (lane & 15) >> 2, pp = lane & 3, pair = ct >> 1, c = ct & 1;
  int o1, r1, o2, r2;
  if (ORD == 0) { o1 = g >> 1; r1 = 4 * (g & 1) + q; o2 = o1 + 2; r2 = r1; }
  else { o1 = 2 * (g >> 1); r1 = 4 * (g & 1) + q; o2 = o1 + 1; r2 = r1; }
  const s16x4 lo = trd(kbase + (o1 * NP + pair) * 512 + r1 * 64 + ((c ^ (r1 >> 2) ^ o1) & 1) * 32 + pp * 8);
  const s16x4 hi = trd(kbase + (o2 * NP + pair) * 512 + r2 * 64 + ((c ^ (r2 >> 2) ^ o2) & 1) * 32 + pp * 8);
  return __builtin_shufflevector(lo, hi, 0, 1, 2, 3, 4, 5, 6, 7);
}

template <int NT, int S0, int S1, int NX, int PD, bool VAL, class XL, class OFF, class HOOK>
DI void gemm_lds(f32x4 (&acc)[NT], bf16x8 (&X)[NX], const LAS char* wb, XL xload, OFF off, HOOK hook, rsrc_t rws, bool& dead) {
  bf16x8 wf[2][NT];
#pragma unroll
  for (int c = 0; c < NT; ++c) wf[0][c] = ldsfrag(wb + off(S0) + c * 1024);
  SBAR();
#pragma unroll
  for (int s = S0; s < S1; ++s) {
    const int cur = (s - S0) & 1;
    if (PD > 0) {
#pragma unroll
      for (int e = 0; e < 2; ++e) { const int ix = PD + 2 * s + e; if (ix < NX) X[ix] = xload(ix); }
    }
    hook(s);
    __builtin_amdgcn_s_waitcnt(0xC07F);
    SBAR();
#pragma unroll
    for (int c = 0; c < NT; ++c) {
      if (s + 1 < S1) wf[cur ^ 1][c] = ldsfrag(wb + off(s + 1) + c * 1024);
      acc[c] = mfma16(wf[cur][c], X[s], acc[c]);
    }
#pragma unroll
    for (int c = 0; c < NT; ++c) {
      __builtin_amdgcn_sched_group_barrier(0x008, 1, 0);
      if (s + 1 < S1 && 2 * c < NT) __builtin_amdgcn_sched_group_barrier(0x100, 2, 0);
    }
    SBAR();
  }
}

#define STAMP(i) do {} while (0)
#define RT(i) do {} while (0)
#define STAMP_OUT() do {} while (0)

DI void stats_load(rsrc_t rws, unsigned base, int lane, u32x2 (&pv)[4]) {
  const int tok = lane & 15, g = lane >> 4;
#pragma unroll
  for (int i = 0; i < 4; ++i) pv[i] = ld64(rws, (unsigned)((4 * g + i) * 128 + tok * 8), base);
}
DI void stats_from_partials(const u32x2 (&pv)[4], float& mean, float& var) {
  float s = 0.f;
#pragma unroll
  for (int i = 0; i < 4; ++i) s += __uint_as_float(pv[i].x);
  s = quad_sum(s);
  mean = s * (1.0f / 512.0f);
  float m2 = 0.f;
#pragma unroll
  for (int i = 0; i < 4; ++i) { const float d = __uint_as_float(pv[i].x) * (1.0f / 32.0f) - mean; m2 += __uint_as_float(pv[i].y) + 32.0f * d * d; }
  m2 = quad_sum(m2);
  var = m2 * (1.0f / 512.0f);
}

DI void publish_x(rsrc_t rws, const float (&v)[8], unsigned xoff, unsigned stoff, int lane, u32x4& packed) {
  packed.x = cvtpk(v[0], v[1]); packed.y = cvtpk(v[2], v[3]); packed.z = cvtpk(v[4], v[5]); packed.w = cvtpk(v[6], v[7]);
  st128(packed, rws, (unsigned)lane * 16u, xoff);
  float r[8] = {bflo(packed.x), bfhi(packed.x), bflo(packed.y), bfhi(packed.y), bflo(packed.z), bfhi(packed.z), bflo(packed.w), bfhi(packed.w)};
  float s = 0.f;
#pragma unroll
  for (int i = 0; i < 8; ++i) s += r[i];
  s = quad_sum(s);
  const float mu = s * (1.0f / 32.0f);
  float m2 = 0.f;
#pragma unroll
  for (int i = 0; i < 8; ++i) { const float d = r[i] - mu; m2 += d * d; }
  m2 = quad_sum(m2);
  if (lane < 16) { u32x2 o = {__float_as_uint(s), __float_as_uint(m2)}; st64(o, rws, (unsigned)lane * 8u, stoff); }
}

template <bool FULL, class HOOK>
DI u32x4 attn_wave(const LAS char* lds, int hd, int qi, int jmin, int lane, HOOK hook) {
  const int ql = lane & 31, h = lane >> 5;
  const bf16x8 Q = ldsfrag(lds + A_Q + (hd * 2 + h) * 2048 + (32 * qi + ql) * 16);
  const LAS char* kb = lds + A_K + (hd * 2 + h) * PLN + (32 * qi + ql) * 16;
  const int G = lane >> 4, q4 = (lane & 15) >> 2, pp = lane & 3;
  const LAS char* vb = lds + ((G & 1) ? A_ONE : A_V + (hd * 2 + (pp >> 1)) * PLN) + (32 * qi + 4 * h + q4) * 16 + (pp & 1) * 8;
  constexpr float NEG = -1e30f;
  const f32x16 zero16 = {0.f,0.f,0.f,0.f,0.f,0.f,0.f,0.f,0.f,0.f,0.f,0.f,0.f,0.f,0.f,0.f};
  f32x16 biasD;
  {
    const LAS f32x4* bt = (const LAS f32x4*)(lds + T_MASK + lane * 16);
#pragma unroll
    for (int q = 0; q < 4; ++q) { const f32x4 v = bt[q * 64]; biasD[4 * q] = v[0]; biasD[4 * q + 1] = v[1]; biasD[4 * q + 2] = v[2]; biasD[4 * q + 3] = v[3]; }
  }
  float m0 = NEG, m1 = NEG;
  {
    bf16x8 ka = ldsfrag(kb + 8 * 512), kn = ka;
    if (FULL || 7 >= jmin) kn = ldsfrag(kb + 7 * 512);
    f32x16 sa = mfma32(ka, Q, biasD), sb = sa;
    SBAR();
#pragma unroll
    for (int j = 8; j >= 0; --j) {
      f32x16& cur = (j & 1) ? sb : sa; f32x16& nxt = (j & 1) ? sa : sb;
      if (j >= 1 && (FULL || j - 1 >= jmin)) {
        const bf16x8 kj = kn;
        if (j >= 2 && (FULL || j - 2 >= jmin)) kn = ldsfrag(kb + (j - 2) * 512);
        if (j - 1 == 0) { f32x16 bf;
#pragma unroll
          for (int r = 0; r < 16; ++r) bf[r] = NEG - biasD[r];
          nxt = mfma32(kj, Q, bf); }
        else nxt = mfma32(kj, Q, zero16);
      }
      if (FULL || j >= jmin) {
#pragma unroll
        for (int r = 0; r < 16; r += 4) { m0 = max3f(m0, cur[r], cur[r + 1]); m1 = max3f(m1, cur[r + 2], cur[r + 3]); }
        asm volatile("" : "+v"(m0), "+v"(m1));
      }
      SBAR();
    }
  }
  const float m = swap32_max(fmaxf(m0, m1));
  f32x16 negm;
#pragma unroll
  for (int r = 0; r < 16; ++r) negm[r] = -m;
  f32x16 o = zero16;
  {
    bf16x8 ka = ldsfrag(kb + 8 * 512), kn = ka;
    if (FULL || 7 >= jmin) kn = ldsfrag(kb + 7 * 512);
    f32x16 sa = mfma32(ka, Q, biasD + negm), sb = sa;
    SBAR();
#pragma unroll
    for (int j = 8; j >= 0; --j) {
      f32x16& cur = (j & 1) ? sb : sa; f32x16& nxt = (j & 1) ? sa : sb;
      const bool have = FULL || j >= jmin;
      hook(j);
      s16x4 v0 = {}, v1 = {}, v2 = {}, v3 = {};
      if (have) { v0 = trd(vb + (32 * j) * 16); v1 = trd(vb + (32 * j + 8) * 16); v2 = trd(vb + (32 * j + 16) * 16); v3 = trd(vb + (32 * j + 24) * 16); }
      if (j >= 1 && (FULL || j - 1 >= jmin)) {
        const bf16x8 kj = kn;
        if (j >= 2 && (FULL || j - 2 >= jmin)) kn = ldsfrag(kb + (j - 2) * 512);
        if (j - 1 == 0) { f32x16 cf;
#pragma unroll
          for (int r = 0; r < 16; ++r) cf[r] = (NEG - biasD[r]) - m;
          nxt = mfma32(kj, Q, cf); }
        else nxt = mfma32(kj, Q, negm);
      }
      if (have) {
        unsigned pk[8];
#pragma unroll
        for (int r = 0; r < 8; ++r) pk[r] = cvtpk(__builtin_amdgcn_exp2f(cur[2 * r]), __builtin_amdgcn_exp2f(cur[2 * r + 1]));
        o = mfma32(__builtin_shufflevector(v0, v1, 0, 1, 2, 3, 4, 5, 6, 7), asfrag(u32x4{pk[0], pk[1], pk[2], pk[3]}), o);
        o = mfma32(__builtin_shufflevector(v2, v3, 0, 1, 2, 3, 4, 5, 6, 7), asfrag(u32x4{pk[4], pk[5], pk[6], pk[7]}), o);
      }
      SBAR();
    }
  }
  const float inv = __builtin_amdgcn_rcpf(o[8]);
  return u32x4{cvtpk(o[0] * inv, o[1] * inv), cvtpk(o[2] * inv, o[3] * inv), cvtpk(o[4] * inv, o[5] * inv), cvtpk(o[6] * inv, o[7] * inv)};
}

__global__ __launch_bounds__(512, 2) void mega(Params P) {
  __shared__ __attribute__((aligned(16))) char lds_[LDS_BYTES];
  const LAS char* lds = (const LAS char*)lds_;
  LAS char* ldsw = (LAS char*)lds_;
  const unsigned ldsb = (unsigned)(size_t)lds_;
  int w = rfl(threadIdx.x >> 6);
  int lane = (int)__builtin_amdgcn_mbcnt_hi(~0u, __builtin_amdgcn_mbcnt_lo(~0u, 0u));
  const int b = blockIdx.x;
  int t = 4 * ((b & 7) >> 1) + (b >> 6), p = 8 * (b & 1) + ((b >> 3) & 7);
  int hh = p >> 3, cg = p & 7;
  int tt = w & 3, kh = w >> 2;
  int tauA = 8 * t + w, tauB = 8 * t + 4 * hh + tt, ksB = 2 * cg + kh, u32i = 2 * t + hh;
  int tok = lane & 15, g = lane >> 4;
  const rsrc_t rws = __builtin_amdgcn_make_buffer_rsrc(P.ws, 0, (int)WS_END, 0x00020000);
  bool dead = false;
  STAMP(0); RT(59);
  if (w == 7) {
    u64 pc;
    asm volatile("s_getpc_b64 %0" : "=s"(pc));
    const rsrc_t rc = __builtin_amdgcn_make_buffer_rsrc((void*)pc, 0, CODE_BYTES, 0x00020000);
    touch8k(rc, (unsigned)lane * 128u, (unsigned)((b >> 3) % 5) * 8192u, ldsb + T_DUMP);
    touch8k(rws, (unsigned)lane << 20, 0u, ldsb + T_DUMP);
  }
  u64 tag = 0;
  u32x4 resid = {0u, 0u, 0u, 0u};
  auto nohook = [](int) INL {};
  auto noload = [](int) INL { return bf16x8{}; };

  int nL = 2;
  asm volatile("" : "+s"(nL));
#pragma unroll 1
  for (int L = 0; L < nL; ++L) {
    asm volatile("" : "+s"(L));
    auto load_wq = [&](int w0, int nw) INL {
      if (w >= w0) for (int sl = w - w0; sl < 16; sl += nw) { const unsigned src = WS_WQT + (unsigned)((p * 16 + (sl ^ (2 * (p & 7)))) * 6) * 1024u, dst = ldsb + MAIN + (unsigned)sl * 6144u;
        dma4k(rws, (unsigned)lane * 16u, src, dst); dma2k(rws, (unsigned)lane * 16u, src + 4096u, dst + 4096u); }
    };
    auto load_w1 = [&](int h0, int h1, int w0, int nw) INL {
      if (p < NP1 && w >= w0) for (int hs = h0 + w - w0; hs < h1; hs += nw) { const int sl = hs >> 1;
        dma4k(rws, (unsigned)lane * 16u, WS_W1T + (unsigned)((p * 16 + (sl ^ (2 * (p & 7)))) * 8 + (hs & 1) * 4) * 1024u, ldsb + L_W1 + (unsigned)hs * 4096u); }
    };
    auto w2_half = [&](int i, int e) INL {
      const int slot = i >= 19 ? i - 19 : i, k = i ^ (p & 3);
      dma4k(rws, (unsigned)lane * 16u, WS_W2T + (unsigned)((cg * 56 + e * 28 + k) * 4) * 1024u, ldsb + MAIN + (unsigned)(slot * 8 + e * 4) * 1024u);
    };
    auto load_w2 = [&](int i0, int i1, int w0, int nw) INL {
      if (w >= w0) for (int q = 2 * i0 + w - w0; q < 2 * i1; q += nw) w2_half(q >> 1, q & 1);
    };
    FRESH();
    {
      bf16x8 X[16];
      float mean = 0.f, var = 0.f;
      const int rotA = 2 * (p & 7);
      f32x4 akv[4];
#pragma unroll
      for (int c = 0; c < 4; ++c) akv[c] = f32x4{0.f, 0.f, 0.f, 0.f};
      if (L == 0) {
        const rsrc_t rx = __builtin_amdgcn_make_buffer_rsrc((void*)P.x, 0, S_ * D_ * 2, 0x00020000);
        if (w < 2) {
          const rsrc_t rq = __builtin_amdgcn_make_buffer_rsrc((void*)P.wqkv, 0, D_ * 3 * D_ * 2, 0x00020000);
#pragma unroll
          for (int jj = 0; jj < 3; ++jj) { const int j = 3 * w + jj; dma16(rq, raw_voff<3>(j, lane, 3072u, (unsigned)p * 64u, 1024u), rfl((unsigned)t * 32u * 3072u), rfl(ldsb + L_PRE + 18 * KB + (unsigned)j * 1024u)); }
        } else if (w < 4) {
          if (p < NP1) {
            const rsrc_t r1 = __builtin_amdgcn_make_buffer_rsrc((void*)P.w1, 0, D_ * F_ * 2, 0x00020000);
#pragma unroll
            for (int jj = 0; jj < 4; ++jj) { const int j = 4 * (w - 2) + jj; dma16(r1, raw_voff<4>(j, lane, 3584u, (unsigned)p * 256u, 64u), rfl((unsigned)t * 32u * 3584u), rfl(ldsb + L_PRE + (unsigned)j * 1024u)); }
          }
        } else if (w == 4) {
          const rsrc_t ro = __builtin_amdgcn_make_buffer_rsrc((void*)P.wo, 0, D_ * D_ * 2, 0x00020000);
#pragma unroll
          for (int j = 0; j < 2; ++j) dma16(ro, raw_voff<1>(j, lane, 1024u, (unsigned)(cg * 128 + (u32i & 1) * 64), 0u), rfl((unsigned)(u32i >> 1) * 32u * 1024u), rfl(ldsb + L_PRE + 8 * KB + (unsigned)j * 1024u));
        } else if (w == 5 || (w == 6 && u32i < 24)) {
          const rsrc_t r2 = __builtin_amdgcn_make_buffer_rsrc((void*)P.w2, 0, F_ * D_ * 2, 0x00020000);
          const int ks = u32i + (w - 5) * 32;
#pragma unroll
          for (int j = 0; j < 4; ++j) dma16(r2, raw_voff<2>(j, lane, 1024u, (unsigned)cg * 128u, 64u), rfl((unsigned)ks * 32u * 1024u), rfl(ldsb + L_PRE + (10 + (w - 5) * 4) * KB + (unsigned)j * 1024u));
        }
#pragma unroll
        for (int r = 0; r < 16; ++r) { const int rr = r ^ (2 * (p & 7)); dma16(rx, (unsigned)lane * 16u, (unsigned)(16 * tauA + rr) * 1024u, ldsb + MAIN + (unsigned)((16 * w + rr) * XROW)); }
        u32x2 epoch_raw = ld64(rws, WS_EPOCH, 0);
        STAMP(23);
        VMWAIT0();
        tag = ((((u64)rfl(epoch_raw.y)) << 32) | rfl(epoch_raw.x)) + 1ull;
        BAR();
        STAMP(1);
        if (w < 6) st128(__builtin_bit_cast(u32x4, raw_frag<3, 0>(lds + L_PRE + 18 * KB, w, lane)), rws, (unsigned)lane * 16u, rfl(WS_WQT + (unsigned)((p * 16 + t) * 6 + w) * 1024u));
        VMWAIT0();
        BAR();
        if (w == 0 && lane == 0) set_flag(rws, FL_WT + (unsigned)(t * 16 + p) * 128u, tag);
        STAMP(2);
        FRESH_V();
        if (p < NP1) st128(__builtin_bit_cast(u32x4, raw_frag<4, 0>(lds + L_PRE, w, lane)), rws, (unsigned)lane * 16u, rfl(WS_W1T + (unsigned)((p * 16 + t) * 8 + w) * 1024u));
        if (w >= 6) st128(__builtin_bit_cast(u32x4, raw_frag<1, 1>(lds + L_PRE + 8 * KB, w - 6, lane)), rws, (unsigned)lane * 16u, rfl(WS_WOT + (unsigned)((cg * 16 + (u32i >> 1)) * 4 + (u32i & 1) * 2 + (w - 6)) * 1024u));
        if (w < 4 || u32i < 24) { const int hs = w >> 2, ks = u32i + hs * 32;
          st128(__builtin_bit_cast(u32x4, raw_frag<2, 0>(lds + L_PRE + (10 + hs * 4) * KB, w & 3, lane)), rws, (unsigned)lane * 16u, rfl(WS_W2T + (unsigned)((cg * 56 + ks) * 4 + (w & 3)) * 1024u)); }
        {
          const LAS char* xp = lds + MAIN + (16 * w + tok) * XROW + g * 8;
#pragma unroll
          for (int s = 0; s < 16; ++s) { const int ks = s ^ rotA; const u32x2 lo = *(const LAS u32x2*)(xp + ks * 64), hi = *(const LAS u32x2*)(xp + ks * 64 + 32); X[s] = asfrag(u32x4{lo.x, lo.y, hi.x, hi.y}); }
          f32x4 s1 = {0.f, 0.f, 0.f, 0.f}, s2 = {0.f, 0.f, 0.f, 0.f};
          const bf16x8 on = ones_frag();
#pragma unroll
          for (int i = 0; i < 16; ++i) { s1 = mfma16(on, X[i], s1); s2 = mfma16(X[i], X[i], s2); }
          mean = s1[0] * (1.0f / 512.0f);
          const int dj = tok - 4 * g;
          const float dv = dj == 0 ? s2[0] : dj == 1 ? s2[1] : dj == 2 ? s2[2] : dj == 3 ? s2[3] : 0.f;
          var = fmaxf(quad_sum(dv) * (1.0f / 512.0f) - mean * mean, 0.f);
        }
        if (w == 0) poll_flags(rws, FL_WT + (unsigned)((lane * 16 + p) * 128), 16, tag, lane, dead);
        BAR();
        STAMP(24);
        load_wq(0, 8);
        unsigned biasv = 0;
        if (w < 2) { if (p < NP1) biasv = P.b1[p * 128 + w * 64 + lane]; } else if (w == 2) biasv = P.b2[cg * 64 + lane];
        {
          const GAS u32x2* rp = (const GAS u32x2*)(P.x + (size_t)(16 * tauB + tok) * D_ + 32 * ksB + 4 * g);
          const u32x2 r0 = rp[0], r1 = rp[4];
          resid = u32x4{r0.x, r0.y, r1.x, r1.y};
        }
        if (w < 3) *(LAS float*)(ldsw + (w < 2 ? T_B1 + (w * 64 + lane) * 4 : T_B2 + lane * 4)) = bf2f(biasv);
        VMWAIT0();
        BAR();
        STAMP(27);
        if (w < 6) {
          f32x4 cs = {0.f, 0.f, 0.f, 0.f};
#pragma unroll
          for (int s = 0; s < 16; ++s) cs = mfma16(ldsfrag(lds + MAIN + (s * 6 + w) * KB + lane * 16), ones_frag(), cs);
          if (tok == 0) *(LAS f32x4*)(ldsw + T_CSQ + (w * 16 + 4 * g) * 4) = cs;
        }
        STAMP(16 + 32 * L);
        gemm_lds<4, 0, 16, 16, 0, false>(akv, X, lds + MAIN + 2 * KB + lane * 16, noload, [](int s) INL { return s * 6 * KB; }, nohook, rws, dead);
        BAR();
      } else {
        STAMP(16 + 32 * L);
        u32x2 pv[4];
        const unsigned xb = WS_X2 + (unsigned)(tauA * 16 + rotA) * 1024u;
        auto xl = [&](int s) INL { return asfrag(ld128(rws, (unsigned)lane * 16u, xb ^ ((unsigned)s << 10))); };
#pragma unroll
        for (int s = 0; s < PDA; ++s) X[s] = xl(s);
        auto hs = [&](int s) INL { if (s == 10) stats_load(rws, WS_ST2 + (unsigned)tauA * 2048u, lane, pv); };
        gemm_lds<4, 0, 16, 16, PDA, false>(akv, X, lds + MAIN + 2 * KB + lane * 16, xl, [](int s) INL { return s * 6 * KB; }, hs, rws, dead);
        stats_from_partials(pv, mean, var);
      }
      FRESH();
      const float rs = __builtin_amdgcn_rsqf(var + EPS), rm = rs * mean;
      auto qkv_pack = [&](const f32x4& a0, const f32x4& a1, int c0, float sc) INL {
        const f32x4 cs0 = *(const LAS f32x4*)(lds + T_CSQ + (c0 * 16 + 4 * g) * 4), cs1 = *(const LAS f32x4*)(lds + T_CSQ + (c0 * 16 + 16 + 4 * g) * 4);
        float v[8];
#pragma unroll
        for (int j = 0; j < 4; ++j) { v[j] = (rs * a0[j] - rm * cs0[j]) * sc; v[4 + j] = (rs * a1[j] - rm * cs1[j]) * sc; }
        auto s0 = __builtin_amdgcn_permlane16_swap(cvtpk(v[0], v[1]), cvtpk(v[4], v[5]), false, false);
        auto s1 = __builtin_amdgcn_permlane16_swap(cvtpk(v[2], v[3]), cvtpk(v[6], v[7]), false, false);
        return u32x4{s0[0], s1[0], s0[1], s1[1]};
      };
      {
        const u32x4 ko = qkv_pack(akv[0], akv[1], 2, 1.0f), vo = qkv_pack(akv[2], akv[3], 4, 1.0f);
        st128(ko, rws, (unsigned)(PLANE * 2048 + TL * 16), WS_KG + (unsigned)((L * 16 + t) * 16 + p) * 8192u);
        st128(vo, rws, (unsigned)(PLANE * 2048 + TL * 16), WS_VG + (unsigned)((L * 16 + t) * 16 + p) * 8192u);
        *(LAS u32x4*)(ldsw + A_K + PLANE * PLN + (256 + TL) * 16) = ko;
        *(LAS u32x4*)(ldsw + A_V + PLANE * PLN + (256 + TL) * 16) = vo;
      }
      STAMP(3 + 32 * L);
      FRESH();
      f32x4 aq[2] = {f32x4{0.f, 0.f, 0.f, 0.f}, f32x4{0.f, 0.f, 0.f, 0.f}};
      gemm_lds<2, 0, 8, 16, 0, false>(aq, X, lds + MAIN + lane * 16, noload, [](int s) INL { return s * 6 * KB; }, nohook, rws, dead);
      VMWAIT0();
      BAR();
      if (w == 0 && lane == 0) set_flag(rws, flag_row(0, K_KV) + (unsigned)(L * 5 * 256 + t * 16 + p) * 128u, tag);
      gemm_lds<2, 8, 16, 16, 0, false>(aq, X, lds + MAIN + lane * 16, noload, [](int s) INL { return s * 6 * KB; }, nohook, rws, dead);
      STAMP(4 + 32 * L);
      FRESH();
      const u32x4 qo = qkv_pack(aq[0], aq[1], 0, QSCALE);
      if (w == 0) {
        if (L == 0) {
          const int grp = lane >> 4, pg = grp == 0 ? p : cg + 8 * (grp - 1);
          poll_flags(rws, flag_row(0, K_KV) + (unsigned)(((lane & 15) * 16 + pg) * 128), 48, tag, lane, dead);
        } else poll_flags(rws, flag_row(1, K_KV) + (unsigned)(((t - 1 - lane) * 16 + p) * 128), t < 2 ? t : 2, tag, lane, dead);
      }
      BAR();
      STAMP(5 + 32 * L);
      FRESH();
      {
        const int d = 1 + (w >> 2);
        u32x4 hv[4] = {u32x4{}, u32x4{}, u32x4{}, u32x4{}};
        if (t - d >= 0) {
          const unsigned hb = ((w & 2) ? WS_VG : WS_KG) + (unsigned)((L * 16 + t - d) * 16 + p) * 8192u + (unsigned)(w & 1) * 4096u;
#pragma unroll
          for (int i = 0; i < 4; ++i) hv[i] = ld128(rws, (unsigned)lane * 16u, hb + (unsigned)i * 1024u);
        }
        *(LAS u32x4*)(ldsw + A_Q + PLANE * 2048 + TL * 16) = qo;
        if (w * 64 + lane < 400) *(LAS u32x4*)(ldsw + A_ONE + (w * 64 + lane) * 16) = __builtin_bit_cast(u32x4, ones_frag());
        { const int r0 = 4 * (w & 3) + 2 * (w >> 2), key = (r0 & 3) + 8 * (r0 >> 2), dq = (lane & 31) - 4 * (lane >> 5);
          *(LAS f32x2*)(ldsw + T_MASK + (w & 3) * 1024 + lane * 16 + (w >> 2) * 8) = f32x2{key <= dq ? 0.f : -1e30f, key + 1 <= dq ? 0.f : -1e30f}; }
        if (t - d >= 0) {
#pragma unroll
          for (int i = 0; i < 4; ++i) *(LAS u32x4*)(ldsw + ((w & 2) ? A_V : A_K) + (2 * (w & 1) + (i >> 1)) * PLN + ((2 - d) * 128 + (i & 1) * 64 + lane) * 16) = hv[i];
        }
      }
      BAR();
      STAMP(6 + 32 * L);
    }
    FRESH();
    {
      auto hook = [&](int j) INL {
        if (w < 4 && j == 8 - 2 * w && p < NP1) touch8k(rws, (unsigned)lane * 128u, WS_W1T + (unsigned)(p * 128 + (t & 3) * 32 + w * 8) * 1024u, ldsb + T_DUMP);
        if (w < 2 && j == 7 - 6 * w) touch8k(rws, (unsigned)lane * 128u, WS_WOT + (unsigned)(cg * 64 + (t & 3) * 16 + w * 8) * 1024u, ldsb + T_DUMP);
      };
      STAMP(17 + 32 * L);
      u32x4 ov;
      if (t >= 2) ov = attn_wave<true>(lds, w & 1, w >> 1, 0, lane, hook); else ov = attn_wave<false>(lds, w & 1, w >> 1, 8 - 4 * t - (w >> 1), lane, hook);
      FRESH();
      st128(ov, rws, (unsigned)(((lane & 31) >> 4) * 16384 + ((2 * (w & 1) + (lane >> 5)) * 16 + (lane & 15)) * 16), WS_OG + (unsigned)((L * 128 + 8 * t + 2 * (w >> 1)) * 16 + p) * 1024u);
      STAMP(18 + 32 * L);
      if (w < 4) {
#pragma unroll
        for (int i = 0; i < 4; ++i) { const int sl = w + 4 * i; dma4k(rws, (unsigned)lane * 16u, WS_WOT + (unsigned)((cg * 16 + (sl ^ cg)) * 4) * 1024u, ldsb + L_WO + (unsigned)sl * 4096u); }
      }
      VMWAIT0();
      BAR();
      if (w == 0 && lane == 0) set_flag(rws, flag_row(0, K_O) + (unsigned)(L * 5 * 256 + t * 16 + p) * 128u, tag);
      STAMP(7 + 32 * L);
      load_w1(14, 32, 1, 7);
      if (w == 0) poll_flags(rws, flag_row(0, K_O) + (unsigned)(L * 5 * 256 + t * 16 + lane) * 128u, 16, tag, lane, dead);
      BAR();
      STAMP(8 + 32 * L);
    }
    FRESH();
    {
      bf16x8 Xo[8];
      const unsigned xb = WS_OG + (unsigned)((L * 128 + tauB) * 16 + 8 * kh + cg) * 1024u;
      auto xl = [&](int i) INL { return asfrag(ld128(rws, (unsigned)lane * 16u, xb ^ ((unsigned)i << 10))); };
#pragma unroll
      for (int i = 0; i < 8; ++i) Xo[i] = xl(i);
      f32x4 acc[4];
#pragma unroll
      for (int c = 0; c < 4; ++c) acc[c] = f32x4{0.f, 0.f, 0.f, 0.f};
      gemm_lds<4, 0, 8, 8, 0, false>(acc, Xo, lds + L_WO + kh * 32 * KB + lane * 16, xl, [](int s) INL { return s * 4 * KB; }, nohook, rws, dead);
      STAMP(19 + 32 * L);
      FRESH();
      *(LAS f32x4*)(ldsw + MAIN + (w * 2 + 0) * KB + lane * 16) = kh ? acc[0] : acc[2];
      *(LAS f32x4*)(ldsw + MAIN + (w * 2 + 1) * KB + lane * 16) = kh ? acc[1] : acc[3];
      BAR();
      const f32x4 o0 = *(const LAS f32x4*)(lds + MAIN + ((w ^ 4) * 2 + 0) * KB + lane * 16), o1 = *(const LAS f32x4*)(lds + MAIN + ((w ^ 4) * 2 + 1) * KB + lane * 16);
      const f32x4 k0 = kh ? acc[2] : acc[0], k1 = kh ? acc[3] : acc[1];
      float v[8];
      v[0] = k0[0] + o0[0] + bflo(resid.x); v[1] = k0[1] + o0[1] + bfhi(resid.x); v[2] = k0[2] + o0[2] + bflo(resid.y); v[3] = k0[3] + o0[3] + bfhi(resid.y);
      v[4] = k1[0] + o1[0] + bflo(resid.z); v[5] = k1[1] + o1[1] + bfhi(resid.z); v[6] = k1[2] + o1[2] + bflo(resid.w); v[7] = k1[3] + o1[3] + bfhi(resid.w);
      publish_x(rws, v, WS_X1 + (unsigned)((L * 128 + tauB) * 16 + ksB) * 1024u, WS_ST1 + (unsigned)((L * 128 + tauB) * 16 + ksB) * 128u, lane, resid);
      VMWAIT0();
      BAR();
      if (w == 0 && lane == 0) set_flag(rws, flag_row(0, K_X1) + (unsigned)(L * 5 * 256 + t * 16 + p) * 128u, tag);
      STAMP(9 + 32 * L);
      load_w1(0, 14, 1, 7);
      if (w == 0) poll_flags(rws, flag_row(0, K_X1) + (unsigned)(L * 5 * 256 + t * 16 + lane) * 128u, 16, tag, lane, dead);
      VMWAIT0();
      BAR();
      STAMP(10 + 32 * L);
    }
    FRESH();
    if (p < NP1) {
      bf16x8 X1[16];
      u32x2 pv1[4];
      const unsigned xb = WS_X1 + (unsigned)((L * 128 + tauA) * 16 + 2 * (p & 7)) * 1024u;
      auto xl = [&](int s) INL { return asfrag(ld128(rws, (unsigned)lane * 16u, xb ^ ((unsigned)s << 10))); };
#pragma unroll
      for (int s = 0; s < PDA; ++s) X1[s] = xl(s);
      if (L == 0) {
        f32x4 cs = {0.f, 0.f, 0.f, 0.f};
#pragma unroll
        for (int s = 0; s < 16; ++s) cs = mfma16(ldsfrag(lds + L_W1 + (s * 8 + w) * KB + lane * 16), ones_frag(), cs);
        if (tok == 0) *(LAS f32x4*)(ldsw + T_CS1 + (w * 16 + 4 * g) * 4) = cs;
      }
      STAMP(26 + 32 * L);
      f32x4 acc[8];
#pragma unroll
      for (int c = 0; c < 8; ++c) acc[c] = f32x4{0.f, 0.f, 0.f, 0.f};
      auto hook = [&](int s) INL {
        if (s == 13) stats_load(rws, WS_ST1 + (unsigned)(L * 128 + tauA) * 2048u, lane, pv1);
      };
      gemm_lds<8, 0, 16, 16, PDA, false>(acc, X1, lds + L_W1 + lane * 16, xl, [](int s) INL { return s * 8 * KB; }, hook, rws, dead);
      float mean1, var1;
      stats_from_partials(pv1, mean1, var1);
      if (w < 6) w2_half(w >> 1, w & 1);
      BAR();
      STAMP(11 + 32 * L);
      FRESH_V();
      const float r1 = __builtin_amdgcn_rsqf(var1 + EPS), r2 = __builtin_amdgcn_rsqf(var1 * r1 * r1 + EPS), rs = r1 * r2, rm = rs * mean1;
      constexpr float C1 = -2.0f * 0.7978845608028654f * 1.4426950408889634f, C2 = C1 * 0.044715f;
      const f32x2 rs2 = {rs, rs}, nrm2 = {-rm, -rm}, c1v = {C1, C1}, c2v = {C2, C2}, one2 = {1.0f, 1.0f};
#pragma unroll
      for (int u = 0; u < 4; ++u) {
        f32x2 av[4], e[4];
#pragma unroll
        for (int hf = 0; hf < 2; ++hf) {
          const int c = 2 * u + hf;
          const f32x4 cs = *(const LAS f32x4*)(lds + T_CS1 + (c * 16 + 4 * g) * 4), bb = *(const LAS f32x4*)(lds + T_B1 + (c * 16 + 4 * g) * 4);
          av[2 * hf] = rs2 * f32x2{acc[c][0], acc[c][1]} + (nrm2 * f32x2{cs[0], cs[1]} + f32x2{bb[0], bb[1]});
          av[2 * hf + 1] = rs2 * f32x2{acc[c][2], acc[c][3]} + (nrm2 * f32x2{cs[2], cs[3]} + f32x2{bb[2], bb[3]});
        }
#pragma unroll
        for (int i = 0; i < 4; ++i) e[i] = av[i] * (c2v * (av[i] * av[i]) + c1v);
#pragma unroll
        for (int i = 0; i < 4; ++i) e[i] = f32x2{__builtin_amdgcn_exp2f(e[i].x), __builtin_amdgcn_exp2f(e[i].y)} + one2;
#pragma unroll
        for (int i = 0; i < 4; ++i) e[i] = av[i] * f32x2{__builtin_amdgcn_rcpf(e[i].x), __builtin_amdgcn_rcpf(e[i].y)};
        st128(u32x4{cvtpk2(e[0]), cvtpk2(e[1]), cvtpk2(e[2]), cvtpk2(e[3])}, rws, (unsigned)lane * 16u, WS_HG + (unsigned)((L * 128 + tauA) * 56 + 4 * p + u) * 1024u);
        SBAR();
        w2_half(3 + 4 * u + (w >> 1), w & 1);
        SBAR();
      }
      STAMP(20 + 32 * L);
      asm volatile("s_waitcnt vmcnt(4)" ::: "memory");
      BAR();
      if (w == 0 && lane == 0) set_flag(rws, flag_row(0, K_H) + (unsigned)(L * 5 * 256 + t * 16 + p) * 128u, tag);
    } else {
      if (L == 1 && b == 255 && w == 0) {
        bool d2 = dead;
#pragma unroll 1
        for (int i = 0; i < 4; ++i) poll_flags(rws, FL_WT + (unsigned)(i * 64 + lane) * 128u, 64, tag, lane, d2);
        if (lane == 0) set_flag(rws, WS_EPOCH, tag);
      }
      if (w >= 1) touch8k(rws, (unsigned)lane * 128u, WS_W2T + (unsigned)(cg * 224 + (t & 3) * 56 + (w - 1) * 8) * 1024u, ldsb + T_DUMP);
      load_w2(0, 19, 0, 8);
    }
    FRESH();
    {
      STAMP(12 + 32 * L);
      if (w == 0) poll_flags(rws, flag_row(0, K_H) + (unsigned)(L * 5 * 256 + t * 16 + lane) * 128u, NP1, tag, lane, dead);
      VMWAIT0();
      BAR();
      STAMP(13 + 32 * L);
      bf16x8 Xh[28];
      const unsigned xb = WS_HG + (unsigned)((L * 128 + tauB) * 56 + 28 * kh + (p & 3)) * 1024u;
      auto xl = [&](int i) INL { return asfrag(ld128(rws, (unsigned)lane * 16u, (xb ^ ((unsigned)(i & 3) << 10)) + (unsigned)(i & ~3) * 1024u)); };
#pragma unroll
      for (int i = 0; i < PDB; ++i) Xh[i] = xl(i);
      f32x4 acc[4];
#pragma unroll
      for (int c = 0; c < 4; ++c) acc[c] = f32x4{0.f, 0.f, 0.f, 0.f};
      gemm_lds<4, 0, 9, 28, PDB, false>(acc, Xh, lds + MAIN + kh * 4 * KB + lane * 16, xl, [](int s) INL { return s * 8 * KB; }, nohook, rws, dead);
      STAMP(21 + 32 * L);
      BAR();
      load_w2(19, 28, 0, 8);
      gemm_lds<4, 9, 19, 28, PDB, false>(acc, Xh, lds + MAIN + kh * 4 * KB + lane * 16, xl, [](int s) INL { return s * 8 * KB; }, nohook, rws, dead);
      VMWAIT0();
      BAR();
      STAMP(22 + 32 * L);
      auto hookq = [&](int s) INL { if (L == 0 && w < 3 && s == 19 + 2 * w) touch8k(rws, (unsigned)lane * 128u, WS_WQT + (unsigned)(p * 96 + (t & 3) * 24 + w * 8) * 1024u, ldsb + T_DUMP); };
      gemm_lds<4, 19, 28, 28, PDB, false>(acc, Xh, lds + MAIN + kh * 4 * KB + lane * 16, xl, [](int s) INL { return (s - 19) * 8 * KB; }, hookq, rws, dead);
      STAMP(14 + 32 * L);
      FRESH();
      *(LAS f32x4*)(ldsw + L_RED2 + (w * 2 + 0) * KB + lane * 16) = kh ? acc[0] : acc[2];
      *(LAS f32x4*)(ldsw + L_RED2 + (w * 2 + 1) * KB + lane * 16) = kh ? acc[1] : acc[3];
      BAR();
      const f32x4 o0 = *(const LAS f32x4*)(lds + L_RED2 + ((w ^ 4) * 2 + 0) * KB + lane * 16), o1 = *(const LAS f32x4*)(lds + L_RED2 + ((w ^ 4) * 2 + 1) * KB + lane * 16);
      const f32x4 k0 = kh ? acc[2] : acc[0], k1 = kh ? acc[3] : acc[1];
      const f32x4 b0 = *(const LAS f32x4*)(lds + T_B2 + (kh * 32 + 4 * g) * 4), b1v = *(const LAS f32x4*)(lds + T_B2 + (kh * 32 + 16 + 4 * g) * 4);
      float v[8];
      v[0] = k0[0] + o0[0] + b0[0] + bflo(resid.x); v[1] = k0[1] + o0[1] + b0[1] + bfhi(resid.x); v[2] = k0[2] + o0[2] + b0[2] + bflo(resid.y); v[3] = k0[3] + o0[3] + b0[3] + bfhi(resid.y);
      v[4] = k1[0] + o1[0] + b1v[0] + bflo(resid.z); v[5] = k1[1] + o1[1] + b1v[1] + bfhi(resid.z); v[6] = k1[2] + o1[2] + b1v[2] + bflo(resid.w); v[7] = k1[3] + o1[3] + b1v[3] + bfhi(resid.w);
      if (L == 1) {
        const unsigned a0 = cvtpk(v[0], v[1]), a1 = cvtpk(v[2], v[3]), c0 = cvtpk(v[4], v[5]), c1 = cvtpk(v[6], v[7]);
        auto s0 = __builtin_amdgcn_permlane16_swap(a0, c0, false, false);
        auto s1 = __builtin_amdgcn_permlane16_swap(a1, c1, false, false);
        const u32x4 o = {s0[0], s1[0], s0[1], s1[1]};
        const int choff = (g & 1) * 16 + (g >> 1) * 8;
        { const rsrc_t ro = __builtin_amdgcn_make_buffer_rsrc((void*)P.out, 0, S_ * D_ * 2, 0x00020000);
          __builtin_amdgcn_raw_buffer_store_b128(o, ro, (unsigned)(((16 * tauB + tok) * D_ + 32 * ksB + choff) * 2), 0, OUT_AUX); }
        STAMP(15 + 32 * L); RT(60); STAMP_OUT();
        break;
      }
      publish_x(rws, v, WS_X2 + (unsigned)(tauB * 16 + ksB) * 1024u, WS_ST2 + (unsigned)(tauB * 16 + ksB) * 128u, lane, resid);
      VMWAIT0();
      BAR();
      if (w == 0 && lane == 0) set_flag(rws, flag_row(0, K_X2) + (unsigned)(t * 16 + p) * 128u, tag);
      load_wq(1, 7);
      if (w == 0) poll_flags(rws, flag_row(0, K_X2) + (unsigned)(t * 16 + lane) * 128u, 16, tag, lane, dead);
      VMWAIT0();
      BAR();
      STAMP(15 + 32 * L);
    }
  }
}

extern "C" void kernel_launch(void* const* d_in, const int* in_sizes, int n_in, void* d_out, int out_size, void* d_ws, size_t ws_size, hipStream_t stream) {
  Params P;
  P.x = (const u16*)d_in[0]; P.wqkv = (const u16*)d_in[1]; P.wo = (const u16*)d_in[2];
  P.w1 = (const u16*)d_in[4]; P.b1 = (const u16*)d_in[5]; P.w2 = (const u16*)d_in[6]; P.b2 = (const u16*)d_in[7];
  P.out = (u16*)d_out; P.ws = (unsigned char*)d_ws;
  mega<<<256, 512, 0, stream>>>(P);
}
```

```cpp
#include <hip/hip_runtime.h>
#include <stdint.h>

typedef unsigned short u16;
typedef unsigned long long u64;
typedef short bf16x8 __attribute__((ext_vector_type(8)));
typedef short s16x4 __attribute__((ext_vector_type(4)));
typedef float f32x2 __attribute__((ext_vector_type(2)));
typedef float f32x4 __attribute__((ext_vector_type(4)));
typedef float f32x16 __attribute__((ext_vector_type(16)));
typedef unsigned u32x4 __attribute__((ext_vector_type(4)));
typedef unsigned u32x2 __attribute__((ext_vector_type(2)));
typedef __bf16 bf16x2_t __attribute__((ext_vector_type(2)));
#define LAS __attribute__((address_space(3)))
#define GAS __attribute__((address_space(1)))
#define DI __device__ __forceinline__
#define INL __attribute__((always_inline))
typedef __amdgpu_buffer_rsrc_t rsrc_t;

constexpr int S_ = 2048, D_ = 512, F_ = 1792;
constexpr float EPS = 1e-5f;
constexpr float QSCALE = 0.25f * 1.4426950408889634f;
constexpr int NP1 = 14;

constexpr unsigned WS_EPOCH = 0, WS_ERR = 128, WS_FLAGS = 4096;
enum { K_KV = 0, K_O = 1, K_X1 = 2, K_H = 3, K_X2 = 4 };
constexpr unsigned flag_row(int L, int kind) { return WS_FLAGS + (unsigned)((L * 5 + kind) * 256 * 128); }
constexpr unsigned FL_WT = flag_row(1, K_X2);
constexpr unsigned WS_WQT = 0x60000;
constexpr unsigned WS_WOT = WS_WQT + 16 * 96 * 1024;
constexpr unsigned WS_W1T = WS_WOT + 8 * 64 * 1024;
constexpr unsigned WS_W2T = WS_W1T + NP1 * 128 * 1024;
constexpr unsigned WS_KG = WS_W2T + 8 * 224 * 1024;
constexpr unsigned WS_VG = WS_KG + 2 * 256 * 8192;
constexpr unsigned WS_OG = WS_VG + 2 * 256 * 8192;
constexpr unsigned WS_X1 = WS_OG + 2 * 128 * 16 * 1024;
constexpr unsigned WS_ST1 = WS_X1 + 2 * 128 * 16 * 1024;
constexpr unsigned WS_HG = WS_ST1 + 2 * 128 * 16 * 128;
constexpr unsigned WS_X2 = WS_HG + 2 * 128 * 56 * 1024;
constexpr unsigned WS_ST2 = WS_X2 + 128 * 16 * 1024;
constexpr unsigned WS_DBG = WS_ST2 + 128 * 16 * 128;
constexpr unsigned WS_END = WS_DBG + 256 * 8 * 128 * 8;

constexpr int KB = 1024;
constexpr int CODE_BYTES = 36 * 1024;
constexpr int LDS_BYTES = 163840;
constexpr int T_CSQ = 0;
constexpr int T_CS1 = 512;
constexpr int T_B1 = 1024;
constexpr int T_B2 = 1536;
constexpr int T_DUMP = 1792;
constexpr int MAIN = 2048;
constexpr int PLN = 384 * 16 + 64;
constexpr int A_K = MAIN + 96 * KB, A_V = A_K + 4 * PLN, A_Q = MAIN, A_ONE = A_Q + 4 * 2048;
static_assert(A_V + 4 * PLN <= LDS_BYTES && A_ONE + 6400 <= MAIN + 16 * KB, "attention buffers");
constexpr int T_MASK = MAIN + 148 * KB;
static_assert(A_V + 4 * PLN <= T_MASK && T_MASK + 4096 <= LDS_BYTES, "mask table");
constexpr int L_W1 = MAIN + 30 * KB;
constexpr int L_WO = MAIN + 16 * KB;
constexpr int L_PRE = MAIN + 132 * KB;
constexpr int XROW = 1040;
static_assert(128 * XROW <= 132 * KB && L_PRE + 24 * KB <= LDS_BYTES, "prologue scratch");
constexpr int L_RED2 = MAIN + 96 * KB;
static_assert(L_W1 + 128 * KB <= LDS_BYTES && L_WO + 64 * KB <= LDS_BYTES, "images");

template <int V> struct IC { static constexpr int value = V; };
struct Params {
  const u16 *x, *wqkv, *wo, *w1, *b1, *w2, *b2;
  u16* out;
  unsigned char* ws;
};

DI float bf2f(unsigned v) { return __uint_as_float(v << 16); }
DI float bflo(unsigned v) { return __uint_as_float(v << 16); }
DI float bfhi(unsigned v) { return __uint_as_float(v & 0xffff0000u); }
DI unsigned cvtpk(float lo, float hi) { f32x2 v = {lo, hi}; bf16x2_t b = __builtin_convertvector(v, bf16x2_t); return __builtin_bit_cast(unsigned, b); }
DI unsigned cvtpk2(f32x2 v) { bf16x2_t b = __builtin_convertvector(v, bf16x2_t); return __builtin_bit_cast(unsigned, b); }
DI f32x4 mfma16(bf16x8 a, bf16x8 b, f32x4 c) { return __builtin_amdgcn_mfma_f32_16x16x32_bf16(a, b, c, 0, 0, 0); }
DI f32x16 mfma32(bf16x8 a, bf16x8 b, f32x16 c) { return __builtin_amdgcn_mfma_f32_32x32x16_bf16(a, b, c, 0, 0, 0); }
DI bf16x8 ones_frag() { unsigned o = 0x3F803F80u; asm volatile("" : "+v"(o)); return __builtin_bit_cast(bf16x8, u32x4{o, o, o, o}); }
DI bf16x8 asfrag(u32x4 v) { return __builtin_bit_cast(bf16x8, v); }
DI s16x4 trd(const LAS char* p) { return __builtin_bit_cast(s16x4, __builtin_amdgcn_ds_read_tr16_b64_v4i16((LAS s16x4*)p)); }
DI bf16x8 ldsfrag(const LAS char* p) { return *(const LAS bf16x8*)p; }
DI float max3f(float a, float b, float c) { return __builtin_fmaxf(__builtin_fmaxf(a, b), c); }
DI float swap32_add(float x) { auto r = __builtin_amdgcn_permlane32_swap(__float_as_uint(x), __float_as_uint(x), false, false); return __uint_as_float(r[0]) + __uint_as_float(r[1]); }
DI float swap16_add(float x) { auto r = __builtin_amdgcn_permlane16_swap(__float_as_uint(x), __float_as_uint(x), false, false); return __uint_as_float(r[0]) + __uint_as_float(r[1]); }
DI float quad_sum(float x) { return swap32_add(swap16_add(x)); }
DI float swap32_max(float x) { auto r = __builtin_amdgcn_permlane32_swap(__float_as_uint(x), __float_as_uint(x), false, false); return fmaxf(__uint_as_float(r[0]), __uint_as_float(r[1])); }

#define FRESH() do { asm volatile("" : "+v"(lane), "+s"(w), "+s"(t), "+s"(p)); tok = lane & 15; g = lane >> 4; hh = p >> 3; cg = p & 7; tt = w & 3; kh = w >> 2; \
  tauA = 8 * t + w; tauB = 8 * t + 4 * hh + tt; ksB = 2 * cg + kh; u32i = 2 * t + hh; } while (0)
#define PLANE ((g & 1) * 2 + (g >> 1))
#define TL (16 * w + tok)
#define FRESH_V() do { asm volatile("" : "+v"(lane)); tok = lane & 15; g = lane >> 4; } while (0)
#define BAR() asm volatile("s_waitcnt lgkmcnt(0)\n\ts_barrier" ::: "memory")
#define VMWAIT0() asm volatile("s_waitcnt vmcnt(0)" ::: "memory")
#define SBAR() __builtin_amdgcn_sched_barrier(0)
constexpr int SC1 = 16;
#define RATEA 2
#define RATEB 1
#define PDA 4
#define PDB 4
#define OUT_AUX 16
#define XAUX 16
DI u32x4 ld128(rsrc_t r, unsigned voff, unsigned soff) { return __builtin_amdgcn_raw_buffer_load_b128(r, voff, soff, XAUX); }
DI u32x2 ld64(rsrc_t r, unsigned voff, unsigned soff) { return __builtin_amdgcn_raw_buffer_load_b64(r, voff, soff, SC1); }
DI void st128(u32x4 v, rsrc_t r, unsigned voff, unsigned soff) { __builtin_amdgcn_raw_buffer_store_b128(v, r, voff + soff, 0, SC1); }
DI void st64(u32x2 v, rsrc_t r, unsigned voff, unsigned soff) { __builtin_amdgcn_raw_buffer_store_b64(v, r, voff + soff, 0, SC1); }
DI unsigned rfl(unsigned x) { return __builtin_amdgcn_readfirstlane(x); }
DI void dma16(rsrc_t r, unsigned voff, unsigned soff, unsigned ldsaddr) {
  asm volatile("s_mov_b32 m0, %0\n\ts_nop 3\n\tbuffer_load_dwordx4 %1, %2, %3 offen lds" :: "s"(rfl(ldsaddr)), "v"(voff), "s"(r), "s"(rfl(soff)) : "memory");
}
DI void dma4k(rsrc_t r, unsigned voff, unsigned soff, unsigned ldsaddr) {
  asm volatile("s_mov_b32 m0, %0\n\ts_nop 3\n\tbuffer_load_dwordx4 %1, %2, %3 offen sc1 lds\n\tbuffer_load_dwordx4 %1, %2, %3 offen offset:1024 sc1 lds\n\t"
               "buffer_load_dwordx4 %1, %2, %3 offen offset:2048 sc1 lds\n\tbuffer_load_dwordx4 %1, %2, %3 offen offset:3072 sc1 lds" :: "s"(rfl(ldsaddr)), "v"(voff), "s"(r), "s"(rfl(soff)) : "memory");
}
DI void dma2k(rsrc_t r, unsigned voff, unsigned soff, unsigned ldsaddr) {
  asm volatile("s_mov_b32 m0, %0\n\ts_nop 3\n\tbuffer_load_dwordx4 %1, %2, %3 offen sc1 lds\n\tbuffer_load_dwordx4 %1, %2, %3 offen offset:1024 sc1 lds" :: "s"(rfl(ldsaddr)), "v"(voff), "s"(r), "s"(rfl(soff)) : "memory");
}
DI void touch8k(rsrc_t r, unsigned voff, unsigned soff, unsigned ldsdump) {
  asm volatile("s_mov_b32 m0, %0\n\ts_nop 3\n\tbuffer_load_dword %1, %2, %3 offen sc1 lds" :: "s"(rfl(ldsdump)), "v"(voff), "s"(r), "s"(rfl(soff)) : "memory");
}

#define POLL_GAP 3
constexpr unsigned SPIN_CAP = 1u << 14;
DI void poll_flags(rsrc_t rws, unsigned voff, int n, u64 tag, int lane, bool& dead) {
  if (dead) return;
  const unsigned vo = lane < n ? voff : WS_EPOCH + 64u;
  const bool idle = lane >= n;
  u32x2 v0, v1, v2;
#define POLL_ISSUE(v) asm volatile("buffer_load_dwordx2 %0, %1, %2, 0 offen sc1" : "=v"(v) : "v"(vo), "s"(rws) : "memory")
#define POLL_OK(v) __all(idle || (v.x == (unsigned)tag && v.y == (unsigned)(tag >> 32)))
  asm volatile("s_waitcnt vmcnt(0)" ::: "memory");
  POLL_ISSUE(v0); __builtin_amdgcn_s_sleep(POLL_GAP);
  POLL_ISSUE(v1); __builtin_amdgcn_s_sleep(POLL_GAP);
  POLL_ISSUE(v2);
  for (unsigned spins = 0;;) {
    asm volatile("s_waitcnt vmcnt(2)" : "+v"(v0) :: "memory");
    if (POLL_OK(v0)) break;
    POLL_ISSUE(v0);
    asm volatile("s_waitcnt vmcnt(2)" : "+v"(v1) :: "memory");
    if (POLL_OK(v1)) break;
    POLL_ISSUE(v1);
    asm volatile("s_waitcnt vmcnt(2)" : "+v"(v2) :: "memory");
    if (POLL_OK(v2)) break;
    POLL_ISSUE(v2);
    if (++spins > SPIN_CAP) { dead = true; break; }
  }
  asm volatile("s_waitcnt vmcnt(0)" : "+v"(v0), "+v"(v1), "+v"(v2) :: "memory");
  if (dead && lane == 0) __builtin_amdgcn_raw_buffer_store_b32(1u, rws, WS_ERR, 0, SC1);
#undef POLL_ISSUE
#undef POLL_OK
}
DI void set_flag(rsrc_t rws, unsigned off, u64 tag) { u32x2 v = {(unsigned)tag, (unsigned)(tag >> 32)}; st64(v, rws, off, 0); }

template <int NP> DI unsigned raw_voff(int j, int lane, unsigned rowpitch, unsigned cb0, unsigned cbs) {
  const int beta = 2 * j + (lane >> 5), oct = beta / NP, pair = beta % NP, r = (lane >> 2) & 7, q4 = lane & 3;
  return (unsigned)(8 * oct + r) * rowpitch + cb0 + (unsigned)pair * cbs + (unsigned)((((q4 >> 1) ^ (r >> 2) ^ oct) & 1) * 32 + (q4 & 1) * 16);
}
template <int NP, int ORD> DI bf16x8 raw_frag(const LAS char* kbase, int ct, int lane) {
  const int g = lane >> 4, q = (lane & 15) >> 2, pp = lane & 3, pair = ct >> 1, c = ct & 1;
  int o1, r1, o2, r2;
  if (ORD == 0) { o1 = g >> 1; r1 = 4 * (g & 1) + q; o2 = o1 + 2; r2 = r1; }
  else { o1 = 2 * (g >> 1); r1 = 4 * (g & 1) + q; o2 = o1 + 1; r2 = r1; }
  const s16x4 lo = trd(kbase + (o1 * NP + pair) * 512 + r1 * 64 + ((c ^ (r1 >> 2) ^ o1) & 1) * 32 + pp * 8);
  const s16x4 hi = trd(kbase + (o2 * NP + pair) * 512 + r2 * 64 + ((c ^ (r2 >> 2) ^ o2) & 1) * 32 + pp * 8);
  return __builtin_shufflevector(lo, hi, 0, 1, 2, 3, 4, 5, 6, 7);
}

template <int NT, int S0, int S1, int NX, int PD, int RATE, class XL, class OFF, class HOOK>
DI void gemm_lds(f32x4 (&acc)[NT], bf16x8 (&X)[NX], const LAS char* wb, XL xload, OFF off, HOOK hook, rsrc_t rws, bool& dead) {
  bf16x8 wf[2][NT];
#pragma unroll
  for (int c = 0; c < NT; ++c) wf[0][c] = ldsfrag(wb + off(S0) + c * 1024);
  SBAR();
#pragma unroll
  for (int s = S0; s < S1; ++s) {
    const int cur = (s - S0) & 1;
    if (PD > 0) {
#pragma unroll
      for (int e = 0; e < RATE; ++e) { const int ix = PD + RATE * s + e; if (ix < NX) X[ix] = xload(ix); }
    }
    hook(s);
    __builtin_amdgcn_s_waitcnt(0xC07F);
    SBAR();
#pragma unroll
    for (int c = 0; c < NT; ++c) {
      if (s + 1 < S1) wf[cur ^ 1][c] = ldsfrag(wb + off(s + 1) + c * 1024);
      acc[c] = mfma16(wf[cur][c], X[s], acc[c]);
    }
#pragma unroll
    for (int c = 0; c < NT; ++c) {
      __builtin_amdgcn_sched_group_barrier(0x008, 1, 0);
      if (s + 1 < S1 && 2 * c < NT) __builtin_amdgcn_sched_group_barrier(0x100, 2, 0);
    }
    SBAR();
  }
}

#define STAMP(i) do {} while (0)
#define RT(i) do {} while (0)
#define STAMP_OUT() do {} while (0)

DI void stats_load(rsrc_t rws, unsigned base, int lane, u32x2 (&pv)[4]) {
  const int tok = lane & 15, g = lane >> 4;
#pragma unroll
  for (int i = 0; i < 4; ++i) pv[i] = ld64(rws, (unsigned)((4 * g + i) * 128 + tok * 8), base);
}
DI void stats_from_partials(const u32x2 (&pv)[4], float& mean, float& var) {
  float s = 0.f;
#pragma unroll
  for (int i = 0; i < 4; ++i) s += __uint_as_float(pv[i].x);
  s = quad_sum(s);
  mean = s * (1.0f / 512.0f);
  float m2 = 0.f;
#pragma unroll
  for (int i = 0; i < 4; ++i) { const float d = __uint_as_float(pv[i].x) * (1.0f / 32.0f) - mean; m2 += __uint_as_float(pv[i].y) + 32.0f * d * d; }
  m2 = quad_sum(m2);
  var = m2 * (1.0f / 512.0f);
}

DI void publish_x(rsrc_t rws, const float (&v)[8], unsigned xoff, unsigned stoff, int lane, u32x4& packed) {
  packed.x = cvtpk(v[0], v[1]); packed.y = cvtpk(v[2], v[3]); packed.z = cvtpk(v[4], v[5]); packed.w = cvtpk(v[6], v[7]);
  st128(packed, rws, (unsigned)lane * 16u, xoff);
  float r[8] = {bflo(packed.x), bfhi(packed.x), bflo(packed.y), bfhi(packed.y), bflo(packed.z), bfhi(packed.z), bflo(packed.w), bfhi(packed.w)};
  float s = 0.f;
#pragma unroll
  for (int i = 0; i < 8; ++i) s += r[i];
  s = quad_sum(s);
  const float mu = s * (1.0f / 32.0f);
  float m2 = 0.f;
#pragma unroll
  for (int i = 0; i < 8; ++i) { const float d = r[i] - mu; m2 += d * d; }
  m2 = quad_sum(m2);
  if (lane < 16) { u32x2 o = {__float_as_uint(s), __float_as_uint(m2)}; st64(o, rws, (unsigned)lane * 8u, stoff); }
}

template <bool FULL, class HOOK>
DI u32x4 attn_wave(const LAS char* lds, int hd, int qi, int jmin, int lane, HOOK hook) {
  const int ql = lane & 31, h = lane >> 5;
  const bf16x8 Q = ldsfrag(lds + A_Q + (hd * 2 + h) * 2048 + (32 * qi + ql) * 16);
  const LAS char* kb = lds + A_K + (hd * 2 + h) * PLN + (32 * qi + ql) * 16;
  const int G = lane >> 4, q4 = (lane & 15) >> 2, pp = lane & 3;
  const LAS char* vb = lds + ((G & 1) ? A_ONE : A_V + (hd * 2 + (pp >> 1)) * PLN) + (32 * qi + 4 * h + q4) * 16 + (pp & 1) * 8;
  constexpr float NEG = -1e30f;
  const f32x16 zero16 = {0.f,0.f,0.f,0.f,0.f,0.f,0.f,0.f,0.f,0.f,0.f,0.f,0.f,0.f,0.f,0.f};
  f32x16 biasD;
  {
    const LAS f32x4* bt = (const LAS f32x4*)(lds + T_MASK + lane * 16);
#pragma unroll
    for (int q = 0; q < 4; ++q) { const f32x4 v = bt[q * 64]; biasD[4 * q] = v[0]; biasD[4 * q + 1] = v[1]; biasD[4 * q + 2] = v[2]; biasD[4 * q + 3] = v[3]; }
  }
  float m0 = NEG, m1 = NEG;
  {
    bf16x8 ka = ldsfrag(kb + 8 * 512), kn = ka;
    if (FULL || 7 >= jmin) kn = ldsfrag(kb + 7 * 512);
    f32x16 sa = mfma32(ka, Q, biasD), sb = sa;
    SBAR();
#pragma unroll
    for (int j = 8; j >= 0; --j) {
      f32x16& cur = (j & 1) ? sb : sa; f32x16& nxt = (j & 1) ? sa : sb;
      if (j >= 1 && (FULL || j - 1 >= jmin)) {
        const bf16x8 kj = kn;
        if (j >= 2 && (FULL || j - 2 >= jmin)) kn = ldsfrag(kb + (j - 2) * 512);
        if (j - 1 == 0) { f32x16 bf;
#pragma unroll
          for (int r = 0; r < 16; ++r) bf[r] = NEG - biasD[r];
          nxt = mfma32(kj, Q, bf); }
        else nxt = mfma32(kj, Q, zero16);
      }
      if (FULL || j >= jmin) {
#pragma unroll
        for (int r = 0; r < 16; r += 4) { m0 = max3f(m0, cur[r], cur[r + 1]); m1 = max3f(m1, cur[r + 2], cur[r + 3]); }
        asm volatile("" : "+v"(m0), "+v"(m1));
      }
      SBAR();
    }
  }
  const float m = swap32_max(fmaxf(m0, m1));
  f32x16 negm;
#pragma unroll
  for (int r = 0; r < 16; ++r) negm[r] = -m;
  f32x16 o = zero16;
  {
    bf16x8 ka = ldsfrag(kb + 8 * 512), kn = ka;
    if (FULL || 7 >= jmin) kn = ldsfrag(kb + 7 * 512);
    f32x16 sa = mfma32(ka, Q, biasD + negm), sb = sa;
    SBAR();
#pragma unroll
    for (int j = 8; j >= 0; --j) {
      f32x16& cur = (j & 1) ? sb : sa; f32x16& nxt = (j & 1) ? sa : sb;
      const bool have = FULL || j >= jmin;
      hook(j);
      s16x4 v0 = {}, v1 = {}, v2 = {}, v3 = {};
      if (have) { v0 = trd(vb + (32 * j) * 16); v1 = trd(vb + (32 * j + 8) * 16); v2 = trd(vb + (32 * j + 16) * 16); v3 = trd(vb + (32 * j + 24) * 16); }
      if (j >= 1 && (FULL || j - 1 >= jmin)) {
        const bf16x8 kj = kn;
        if (j >= 2 && (FULL || j - 2 >= jmin)) kn = ldsfrag(kb + (j - 2) * 512);
        if (j - 1 == 0) { f32x16 cf;
#pragma unroll
          for (int r = 0; r < 16; ++r) cf[r] = (NEG - biasD[r]) - m;
          nxt = mfma32(kj, Q, cf); }
        else nxt = mfma32(kj, Q, negm);
      }
      if (have) {
        unsigned pk[8];
#pragma unroll
        for (int r = 0; r < 8; ++r) pk[r] = cvtpk(__builtin_amdgcn_exp2f(cur[2 * r]), __builtin_amdgcn_exp2f(cur[2 * r + 1]));
        o = mfma32(__builtin_shufflevector(v0, v1, 0, 1, 2, 3, 4, 5, 6, 7), asfrag(u32x4{pk[0], pk[1], pk[2], pk[3]}), o);
        o = mfma32(__builtin_shufflevector(v2, v3, 0, 1, 2, 3, 4, 5, 6, 7), asfrag(u32x4{pk[4], pk[5], pk[6], pk[7]}), o);
      }
      SBAR();
    }
  }
  const float inv = __builtin_amdgcn_rcpf(o[8]);
  return u32x4{cvtpk(o[0] * inv, o[1] * inv), cvtpk(o[2] * inv, o[3] * inv), cvtpk(o[4] * inv, o[5] * inv), cvtpk(o[6] * inv, o[7] * inv)};
}

__global__ __launch_bounds__(512, 2) void mega(Params P) {
  __shared__ __attribute__((aligned(16))) char lds_[LDS_BYTES];
  const LAS char* lds = (const LAS char*)lds_;
  LAS char* ldsw = (LAS char*)lds_;
  const unsigned ldsb = (unsigned)(size_t)lds_;
  int w = rfl(threadIdx.x >> 6);
  int lane = (int)__builtin_amdgcn_mbcnt_hi(~0u, __builtin_amdgcn_mbcnt_lo(~0u, 0u));
  const int b = blockIdx.x;
  int t = 4 * ((b & 7) >> 1) + (b >> 6), p = 8 * (b & 1) + ((b >> 3) & 7);
  int hh = p >> 3, cg = p & 7;
  int tt = w & 3, kh = w >> 2;
  int tauA = 8 * t + w, tauB = 8 * t + 4 * hh + tt, ksB = 2 * cg + kh, u32i = 2 * t + hh;
  int tok = lane & 15, g = lane >> 4;
  const rsrc_t rws = __builtin_amdgcn_make_buffer_rsrc(P.ws, 0, (int)WS_END, 0x00020000);
  bool dead = false;
  STAMP(0); RT(59);
  if (w == 7) {
    u64 pc;
    asm volatile("s_getpc_b64 %0" : "=s"(pc));
    const rsrc_t rc = __builtin_amdgcn_make_buffer_rsrc((void*)pc, 0, CODE_BYTES, 0x00020000);
    touch8k(rc, (unsigned)lane * 128u, (unsigned)((b >> 3) % 5) * 8192u, ldsb + T_DUMP);
    touch8k(rws, (unsigned)lane << 20, 0u, ldsb + T_DUMP);
  }
  u64 tag = 0;
  u32x4 resid = {0u, 0u, 0u, 0u};
  auto nohook = [](int) INL {};
  auto noload = [](int) INL { return bf16x8{}; };

  int nL = 2;
  asm volatile("" : "+s"(nL));
#pragma unroll 1
  for (int L = 0; L < nL; ++L) {
    asm volatile("" : "+s"(L));
    auto load_wq = [&](int w0, int nw) INL {
      if (w >= w0) for (int sl = w - w0; sl < 16; sl += nw) { const unsigned src = WS_WQT + (unsigned)((p * 16 + (sl ^ (2 * (p & 7)))) * 6) * 1024u, dst = ldsb + MAIN + (unsigned)sl * 6144u;
        dma4k(rws, (unsigned)lane * 16u, src, dst); dma2k(rws, (unsigned)lane * 16u, src + 4096u, dst + 4096u); }
    };
    auto load_w1 = [&](int h0, int h1, int w0, int nw) INL {
      if (p < NP1 && w >= w0) for (int hs = h0 + w - w0; hs < h1; hs += nw) { const int sl = hs >> 1;
        dma4k(rws, (unsigned)lane * 16u, WS_W1T + (unsigned)((p * 16 + (sl ^ (2 * (p & 7)))) * 8 + (hs & 1) * 4) * 1024u, ldsb + L_W1 + (unsigned)hs * 4096u); }
    };
    auto w2_half = [&](int i, int e) INL {
      const int slot = i >= 19 ? i - 19 : i, k = i ^ (p & 3);
      dma4k(rws, (unsigned)lane * 16u, WS_W2T + (unsigned)((cg * 56 + e * 28 + k) * 4) * 1024u, ldsb + MAIN + (unsigned)(slot * 8 + e * 4) * 1024u);
    };
    auto load_w2 = [&](int i0, int i1, int w0, int nw) INL {
      if (w >= w0) for (int q = 2 * i0 + w - w0; q < 2 * i1; q += nw) w2_half(q >> 1, q & 1);
    };
    FRESH();
    {
      bf16x8 X[16];
      float mean = 0.f, var = 0.f;
      const int rotA = 2 * (p & 7);
      f32x4 akv[4];
#pragma unroll
      for (int c = 0; c < 4; ++c) akv[c] = f32x4{0.f, 0.f, 0.f, 0.f};
      if (L == 0) {
        const rsrc_t rx = __builtin_amdgcn_make_buffer_rsrc((void*)P.x, 0, S_ * D_ * 2, 0x00020000);
        if (w < 2) {
          const rsrc_t rq = __builtin_amdgcn_make_buffer_rsrc((void*)P.wqkv, 0, D_ * 3 * D_ * 2, 0x00020000);
#pragma unroll
          for (int jj = 0; jj < 3; ++jj) { const int j = 3 * w + jj; dma16(rq, raw_voff<3>(j, lane, 3072u, (unsigned)p * 64u, 1024u), rfl((unsigned)t * 32u * 3072u), rfl(ldsb + L_PRE + 18 * KB + (unsigned)j * 1024u)); }
        } else if (w < 4) {
          if (p < NP1) {
            const rsrc_t r1 = __builtin_amdgcn_make_buffer_rsrc((void*)P.w1, 0, D_ * F_ * 2, 0x00020000);
#pragma unroll
            for (int jj = 0; jj < 4; ++jj) { const int j = 4 * (w - 2) + jj; dma16(r1, raw_voff<4>(j, lane, 3584u, (unsigned)p * 256u, 64u), rfl((unsigned)t * 32u * 3584u), rfl(ldsb + L_PRE + (unsigned)j * 1024u)); }
          }
        } else if (w == 4) {
          const rsrc_t ro = __builtin_amdgcn_make_buffer_rsrc((void*)P.wo, 0, D_ * D_ * 2, 0x00020000);
#pragma unroll
          for (int j = 0; j < 2; ++j) dma16(ro, raw_voff<1>(j, lane, 1024u, (unsigned)(cg * 128 + (u32i & 1) * 64), 0u), rfl((unsigned)(u32i >> 1) * 32u * 1024u), rfl(ldsb + L_PRE + 8 * KB + (unsigned)j * 1024u));
        } else if (w == 5 || (w == 6 && u32i < 24)) {
          const rsrc_t r2 = __builtin_amdgcn_make_buffer_rsrc((void*)P.w2, 0, F_ * D_ * 2, 0x00020000);
          const int ks = u32i + (w - 5) * 32;
#pragma unroll
          for (int j = 0; j < 4; ++j) dma16(r2, raw_voff<2>(j, lane, 1024u, (unsigned)cg * 128u, 64u), rfl((unsigned)ks * 32u * 1024u), rfl(ldsb + L_PRE + (10 + (w - 5) * 4) * KB + (unsigned)j * 1024u));
        }
#pragma unroll
        for (int r = 0; r < 16; ++r) { const int rr = r ^ (2 * (p & 7)); dma16(rx, (unsigned)lane * 16u, (unsigned)(16 * tauA + rr) * 1024u, ldsb + MAIN + (unsigned)((16 * w + rr) * XROW)); }
        u32x2 epoch_raw = ld64(rws, WS_EPOCH, 0);
        STAMP(23);
        VMWAIT0();
        tag = ((((u64)rfl(epoch_raw.y)) << 32) | rfl(epoch_raw.x)) + 1ull;
        BAR();
        STAMP(1);
        if (w < 6) st128(__builtin_bit_cast(u32x4, raw_frag<3, 0>(lds + L_PRE + 18 * KB, w, lane)), rws, (unsigned)lane * 16u, rfl(WS_WQT + (unsigned)((p * 16 + t) * 6 + w) * 1024u));
        VMWAIT0();
        BAR();
        if (w == 0 && lane == 0) set_flag(rws, FL_WT + (unsigned)(t * 16 + p) * 128u, tag);
        STAMP(2);
        FRESH_V();
        if (p < NP1) st128(__builtin_bit_cast(u32x4, raw_frag<4, 0>(lds + L_PRE, w, lane)), rws, (unsigned)lane * 16u, rfl(WS_W1T + (unsigned)((p * 16 + t) * 8 + w) * 1024u));
        if (w >= 6) st128(__builtin_bit_cast(u32x4, raw_frag<1, 1>(lds + L_PRE + 8 * KB, w - 6, lane)), rws, (unsigned)lane * 16u, rfl(WS_WOT + (unsigned)((cg * 16 + (u32i >> 1)) * 4 + (u32i & 1) * 2 + (w - 6)) * 1024u));
        if (w < 4 || u32i < 24) { const int hs = w >> 2, ks = u32i + hs * 32;
          st128(__builtin_bit_cast(u32x4, raw_frag<2, 0>(lds + L_PRE + (10 + hs * 4) * KB, w & 3, lane)), rws, (unsigned)lane * 16u, rfl(WS_W2T + (unsigned)((cg * 56 + ks) * 4 + (w & 3)) * 1024u)); }
        {
          const LAS char* xp = lds + MAIN + (16 * w + tok) * XROW + g * 8;
#pragma unroll
          for (int s = 0; s < 16; ++s) { const int ks = s ^ rotA; const u32x2 lo = *(const LAS u32x2*)(xp + ks * 64), hi = *(const LAS u32x2*)(xp + ks * 64 + 32); X[s] = asfrag(u32x4{lo.x, lo.y, hi.x, hi.y}); }
          f32x4 s1 = {0.f, 0.f, 0.f, 0.f}, s2 = {0.f, 0.f, 0.f, 0.f};
          const bf16x8 on = ones_frag();
#pragma unroll
          for (int i = 0; i < 16; ++i) { s1 = mfma16(on, X[i], s1); s2 = mfma16(X[i], X[i], s2); }
          mean = s1[0] * (1.0f / 512.0f);
          const int dj = tok - 4 * g;
          const float dv = dj == 0 ? s2[0] : dj == 1 ? s2[1] : dj == 2 ? s2[2] : dj == 3 ? s2[3] : 0.f;
          var = fmaxf(quad_sum(dv) * (1.0f / 512.0f) - mean * mean, 0.f);
        }
        if (w == 0) poll_flags(rws, FL_WT + (unsigned)((lane * 16 + p) * 128), 16, tag, lane, dead);
        BAR();
        STAMP(24);
        load_wq(0, 8);
        unsigned biasv = 0;
        if (w < 2) { if (p < NP1) biasv = P.b1[p * 128 + w * 64 + lane]; } else if (w == 2) biasv = P.b2[cg * 64 + lane];
        {
          const GAS u32x2* rp = (const GAS u32x2*)(P.x + (size_t)(16 * tauB + tok) * D_ + 32 * ksB + 4 * g);
          const u32x2 r0 = rp[0], r1 = rp[4];
          resid = u32x4{r0.x, r0.y, r1.x, r1.y};
        }
        if (w < 3) *(LAS float*)(ldsw + (w < 2 ? T_B1 + (w * 64 + lane) * 4 : T_B2 + lane * 4)) = bf2f(biasv);
        VMWAIT0();
        BAR();
        STAMP(27);
        if (w < 6) {
          f32x4 cs = {0.f, 0.f, 0.f, 0.f};
#pragma unroll
          for (int s = 0; s < 16; ++s) cs = mfma16(ldsfrag(lds + MAIN + (s * 6 + w) * KB + lane * 16), ones_frag(), cs);
          if (tok == 0) *(LAS f32x4*)(ldsw + T_CSQ + (w * 16 + 4 * g) * 4) = cs;
        }
        STAMP(16 + 32 * L);
        gemm_lds<4, 0, 16, 16, 0, 2>(akv, X, lds + MAIN + 2 * KB + lane * 16, noload, [](int s) INL { return s * 6 * KB; }, nohook, rws, dead);
        BAR();
      } else {
        STAMP(16 + 32 * L);
        u32x2 pv[4];
        const unsigned xb = WS_X2 + (unsigned)(tauA * 16 + rotA) * 1024u;
        auto xl = [&](int s) INL { return asfrag(ld128(rws, (unsigned)lane * 16u, xb ^ ((unsigned)s << 10))); };
#pragma unroll
        for (int s = 0; s < PDA; ++s) X[s] = xl(s);
        auto hs = [&](int s) INL { if (s == 10) stats_load(rws, WS_ST2 + (unsigned)tauA * 2048u, lane, pv); };
        gemm_lds<4, 0, 16, 16, PDA, RATEA>(akv, X, lds + MAIN + 2 * KB + lane * 16, xl, [](int s) INL { return s * 6 * KB; }, hs, rws, dead);
        stats_from_partials(pv, mean, var);
      }
      FRESH();
      const float rs = __builtin_amdgcn_rsqf(var + EPS), rm = rs * mean;
      auto qkv_pack = [&](const f32x4& a0, const f32x4& a1, int c0, float sc) INL {
        const f32x4 cs0 = *(const LAS f32x4*)(lds + T_CSQ + (c0 * 16 + 4 * g) * 4), cs1 = *(const LAS f32x4*)(lds + T_CSQ + (c0 * 16 + 16 + 4 * g) * 4);
        float v[8];
#pragma unroll
        for (int j = 0; j < 4; ++j) { v[j] = (rs * a0[j] - rm * cs0[j]) * sc; v[4 + j] = (rs * a1[j] - rm * cs1[j]) * sc; }
        auto s0 = __builtin_amdgcn_permlane16_swap(cvtpk(v[0], v[1]), cvtpk(v[4], v[5]), false, false);
        auto s1 = __builtin_amdgcn_permlane16_swap(cvtpk(v[2], v[3]), cvtpk(v[6], v[7]), false, false);
        return u32x4{s0[0], s1[0], s0[1], s1[1]};
      };
      {
        const u32x4 ko = qkv_pack(akv[0], akv[1], 2, 1.0f), vo = qkv_pack(akv[2], akv[3], 4, 1.0f);
        st128(ko, rws, (unsigned)(PLANE * 2048 + TL * 16), WS_KG + (unsigned)((L * 16 + t) * 16 + p) * 8192u);
        st128(vo, rws, (unsigned)(PLANE * 2048 + TL * 16), WS_VG + (unsigned)((L * 16 + t) * 16 + p) * 8192u);
        *(LAS u32x4*)(ldsw + A_K + PLANE * PLN + (256 + TL) * 16) = ko;
        *(LAS u32x4*)(ldsw + A_V + PLANE * PLN + (256 + TL) * 16) = vo;
      }
      STAMP(3 + 32 * L);
      FRESH();
      f32x4 aq[2] = {f32x4{0.f, 0.f, 0.f, 0.f}, f32x4{0.f, 0.f, 0.f, 0.f}};
      gemm_lds<2, 0, 8, 16, 0, 2>(aq, X, lds + MAIN + lane * 16, noload, [](int s) INL { return s * 6 * KB; }, nohook, rws, dead);
      VMWAIT0();
      BAR();
      if (w == 0 && lane == 0) set_flag(rws, flag_row(0, K_KV) + (unsigned)(L * 5 * 256 + t * 16 + p) * 128u, tag);
      gemm_lds<2, 8, 16, 16, 0, 2>(aq, X, lds + MAIN + lane * 16, noload, [](int s) INL { return s * 6 * KB; }, nohook, rws, dead);
      STAMP(4 + 32 * L);
      FRESH();
      const u32x4 qo = qkv_pack(aq[0], aq[1], 0, QSCALE);
      if (w == 0) {
        if (L == 0) {
          const int grp = lane >> 4, pg = grp == 0 ? p : cg + 8 * (grp - 1);
          poll_flags(rws, flag_row(0, K_KV) + (unsigned)(((lane & 15) * 16 + pg) * 128), 48, tag, lane, dead);
        } else poll_flags(rws, flag_row(1, K_KV) + (unsigned)(((t - 1 - lane) * 16 + p) * 128), t < 2 ? t : 2, tag, lane, dead);
      }
      BAR();
      STAMP(5 + 32 * L);
      FRESH();
      {
        const int d = 1 + (w >> 2);
        u32x4 hv[4] = {u32x4{}, u32x4{}, u32x4{}, u32x4{}};
        if (t - d >= 0) {
          const unsigned hb = ((w & 2) ? WS_VG : WS_KG) + (unsigned)((L * 16 + t - d) * 16 + p) * 8192u + (unsigned)(w & 1) * 4096u;
#pragma unroll
          for (int i = 0; i < 4; ++i) hv[i] = ld128(rws, (unsigned)lane * 16u, hb + (unsigned)i * 1024u);
        }
        *(LAS u32x4*)(ldsw + A_Q + PLANE * 2048 + TL * 16) = qo;
        if (w * 64 + lane < 400) *(LAS u32x4*)(ldsw + A_ONE + (w * 64 + lane) * 16) = __builtin_bit_cast(u32x4, ones_frag());
        { const int r0 = 4 * (w & 3) + 2 * (w >> 2), key = (r0 & 3) + 8 * (r0 >> 2), dq = (lane & 31) - 4 * (lane >> 5);
          *(LAS f32x2*)(ldsw + T_MASK + (w & 3) * 1024 + lane * 16 + (w >> 2) * 8) = f32x2{key <= dq ? 0.f : -1e30f, key + 1 <= dq ? 0.f : -1e30f}; }
        if (t - d >= 0) {
#pragma unroll
          for (int i = 0; i < 4; ++i) *(LAS u32x4*)(ldsw + ((w & 2) ? A_V : A_K) + (2 * (w & 1) + (i >> 1)) * PLN + ((2 - d) * 128 + (i & 1) * 64 + lane) * 16) = hv[i];
        }
      }
      BAR();
      STAMP(6 + 32 * L);
    }
    FRESH();
    {
      auto hook = [&](int j) INL {
        if (w < 4 && j == 8 - 2 * w && p < NP1) touch8k(rws, (unsigned)lane * 128u, WS_W1T + (unsigned)(p * 128 + (t & 3) * 32 + w * 8) * 1024u, ldsb + T_DUMP);
        if (w < 2 && j == 7 - 6 * w) touch8k(rws, (unsigned)lane * 128u, WS_WOT + (unsigned)(cg * 64 + (t & 3) * 16 + w * 8) * 1024u, ldsb + T_DUMP);
      };
      STAMP(17 + 32 * L);
      u32x4 ov;
      if (t >= 2) ov = attn_wave<true>(lds, w & 1, w >> 1, 0, lane, hook); else ov = attn_wave<false>(lds, w & 1, w >> 1, 8 - 4 * t - (w >> 1), lane, hook);
      FRESH();
      st128(ov, rws, (unsigned)(((lane & 31) >> 4) * 16384 + ((2 * (w & 1) + (lane >> 5)) * 16 + (lane & 15)) * 16), WS_OG + (unsigned)((L * 128 + 8 * t + 2 * (w >> 1)) * 16 + p) * 1024u);
      STAMP(18 + 32 * L);
      if (w < 4) {
#pragma unroll
        for (int i = 0; i < 4; ++i) { const int sl = w + 4 * i; dma4k(rws, (unsigned)lane * 16u, WS_WOT + (unsigned)((cg * 16 + (sl ^ cg)) * 4) * 1024u, ldsb + L_WO + (unsigned)sl * 4096u); }
      }
      VMWAIT0();
      BAR();
      if (w == 0 && lane == 0) set_flag(rws, flag_row(0, K_O) + (unsigned)(L * 5 * 256 + t * 16 + p) * 128u, tag);
      STAMP(7 + 32 * L);
      load_w1(14, 32, 1, 7);
      if (w == 0) poll_flags(rws, flag_row(0, K_O) + (unsigned)(L * 5 * 256 + t * 16 + lane) * 128u, 16, tag, lane, dead);
      BAR();
      STAMP(8 + 32 * L);
    }
    FRESH();
    {
      bf16x8 Xo[8];
      const unsigned xb = WS_OG + (unsigned)((L * 128 + tauB) * 16 + 8 * kh + cg) * 1024u;
      auto xl = [&](int i) INL { return asfrag(ld128(rws, (unsigned)lane * 16u, xb ^ ((unsigned)i << 10))); };
#pragma unroll
      for (int i = 0; i < 8; ++i) Xo[i] = xl(i);
      f32x4 acc[4];
#pragma unroll
      for (int c = 0; c < 4; ++c) acc[c] = f32x4{0.f, 0.f, 0.f, 0.f};
      gemm_lds<4, 0, 8, 8, 0, 2>(acc, Xo, lds + L_WO + kh * 32 * KB + lane * 16, xl, [](int s) INL { return s * 4 * KB; }, nohook, rws, dead);
      STAMP(19 + 32 * L);
      FRESH();
      *(LAS f32x4*)(ldsw + MAIN + (w * 2 + 0) * KB + lane * 16) = kh ? acc[0] : acc[2];
      *(LAS f32x4*)(ldsw + MAIN + (w * 2 + 1) * KB + lane * 16) = kh ? acc[1] : acc[3];
      BAR();
      const f32x4 o0 = *(const LAS f32x4*)(lds + MAIN + ((w ^ 4) * 2 + 0) * KB + lane * 16), o1 = *(const LAS f32x4*)(lds + MAIN + ((w ^ 4) * 2 + 1) * KB + lane * 16);
      const f32x4 k0 = kh ? acc[2] : acc[0], k1 = kh ? acc[3] : acc[1];
      float v[8];
      v[0] = k0[0] + o0[0] + bflo(resid.x); v[1] = k0[1] + o0[1] + bfhi(resid.x); v[2] = k0[2] + o0[2] + bflo(resid.y); v[3] = k0[3] + o0[3] + bfhi(resid.y);
      v[4] = k1[0] + o1[0] + bflo(resid.z); v[5] = k1[1] + o1[1] + bfhi(resid.z); v[6] = k1[2] + o1[2] + bflo(resid.w); v[7] = k1[3] + o1[3] + bfhi(resid.w);
      publish_x(rws, v, WS_X1 + (unsigned)((L * 128 + tauB) * 16 + ksB) * 1024u, WS_ST1 + (unsigned)((L * 128 + tauB) * 16 + ksB) * 128u, lane, resid);
      VMWAIT0();
      BAR();
      if (w == 0 && lane == 0) set_flag(rws, flag_row(0, K_X1) + (unsigned)(L * 5 * 256 + t * 16 + p) * 128u, tag);
      STAMP(9 + 32 * L);
      load_w1(0, 14, 1, 7);
      if (w == 0) poll_flags(rws, flag_row(0, K_X1) + (unsigned)(L * 5 * 256 + t * 16 + lane) * 128u, 16, tag, lane, dead);
      VMWAIT0();
      BAR();
      STAMP(10 + 32 * L);
    }
    FRESH();
    if (p < NP1) {
      bf16x8 X1[16];
      u32x2 pv1[4];
      const unsigned xb = WS_X1 + (unsigned)((L * 128 + tauA) * 16 + 2 * (p & 7)) * 1024u;
      auto xl = [&](int s) INL { return asfrag(ld128(rws, (unsigned)lane * 16u, xb ^ ((unsigned)s << 10))); };
#pragma unroll
      for (int s = 0; s < PDA; ++s) X1[s] = xl(s);
      if (L == 0) {
        f32x4 cs = {0.f, 0.f, 0.f, 0.f};
#pragma unroll
        for (int s = 0; s < 16; ++s) cs = mfma16(ldsfrag(lds + L_W1 + (s * 8 + w) * KB + lane * 16), ones_frag(), cs);
        if (tok == 0) *(LAS f32x4*)(ldsw + T_CS1 + (w * 16 + 4 * g) * 4) = cs;
      }
      STAMP(26 + 32 * L);
      f32x4 acc[8];
#pragma unroll
      for (int c = 0; c < 8; ++c) acc[c] = f32x4{0.f, 0.f, 0.f, 0.f};
      auto hook = [&](int s) INL {
        if (s == 13) stats_load(rws, WS_ST1 + (unsigned)(L * 128 + tauA) * 2048u, lane, pv1);
      };
      gemm_lds<8, 0, 16, 16, PDA, RATEA>(acc, X1, lds + L_W1 + lane * 16, xl, [](int s) INL { return s * 8 * KB; }, hook, rws, dead);
      float mean1, var1;
      stats_from_partials(pv1, mean1, var1);
      if (w < 6) w2_half(w >> 1, w & 1);
      BAR();
      STAMP(11 + 32 * L);
      FRESH_V();
      const float r1 = __builtin_amdgcn_rsqf(var1 + EPS), r2 = __builtin_amdgcn_rsqf(var1 * r1 * r1 + EPS), rs = r1 * r2, rm = rs * mean1;
      constexpr float C1 = -2.0f * 0.7978845608028654f * 1.4426950408889634f, C2 = C1 * 0.044715f;
      const f32x2 rs2 = {rs, rs}, nrm2 = {-rm, -rm}, c1v = {C1, C1}, c2v = {C2, C2}, one2 = {1.0f, 1.0f};
#pragma unroll
      for (int u = 0; u < 4; ++u) {
        f32x2 av[4], e[4];
#pragma unroll
        for (int hf = 0; hf < 2; ++hf) {
          const int c = 2 * u + hf;
          const f32x4 cs = *(const LAS f32x4*)(lds + T_CS1 + (c * 16 + 4 * g) * 4), bb = *(const LAS f32x4*)(lds + T_B1 + (c * 16 + 4 * g) * 4);
          av[2 * hf] = rs2 * f32x2{acc[c][0], acc[c][1]} + (nrm2 * f32x2{cs[0], cs[1]} + f32x2{bb[0], bb[1]});
          av[2 * hf + 1] = rs2 * f32x2{acc[c][2], acc[c][3]} + (nrm2 * f32x2{cs[2], cs[3]} + f32x2{bb[2], bb[3]});
        }
#pragma unroll
        for (int i = 0; i < 4; ++i) e[i] = av[i] * (c2v * (av[i] * av[i]) + c1v);
#pragma unroll
        for (int i = 0; i < 4; ++i) e[i] = f32x2{__builtin_amdgcn_exp2f(e[i].x), __builtin_amdgcn_exp2f(e[i].y)} + one2;
#pragma unroll
        for (int i = 0; i < 4; ++i) e[i] = av[i] * f32x2{__builtin_amdgcn_rcpf(e[i].x), __builtin_amdgcn_rcpf(e[i].y)};
        st128(u32x4{cvtpk2(e[0]), cvtpk2(e[1]), cvtpk2(e[2]), cvtpk2(e[3])}, rws, (unsigned)lane * 16u, WS_HG + (unsigned)((L * 128 + tauA) * 56 + 4 * p + u) * 1024u);
        SBAR();
        w2_half(3 + 4 * u + (w >> 1), w & 1);
        SBAR();
      }
      STAMP(20 + 32 * L);
      asm volatile("s_waitcnt vmcnt(4)" ::: "memory");
      BAR();
      if (w == 0 && lane == 0) set_flag(rws, flag_row(0, K_H) + (unsigned)(L * 5 * 256 + t * 16 + p) * 128u, tag);
    } else {
      if (L == 1 && b == 255 && w == 0) {
        bool d2 = dead;
#pragma unroll 1
        for (int i = 0; i < 4; ++i) poll_flags(rws, FL_WT + (unsigned)(i * 64 + lane) * 128u, 64, tag, lane, d2);
        if (lane == 0) set_flag(rws, WS_EPOCH, tag);
      }
      if (w >= 1) touch8k(rws, (unsigned)lane * 128u, WS_W2T + (unsigned)(cg * 224 + (t & 3) * 56 + (w - 1) * 8) * 1024u, ldsb + T_DUMP);
      load_w2(0, 19, 0, 8);
    }
    FRESH();
    {
      STAMP(12 + 32 * L);
      if (w == 0) poll_flags(rws, flag_row(0, K_H) + (unsigned)(L * 5 * 256 + t * 16 + lane) * 128u, NP1, tag, lane, dead);
      VMWAIT0();
      BAR();
      STAMP(13 + 32 * L);
      bf16x8 Xh[28];
      const unsigned xb = WS_HG + (unsigned)((L * 128 + tauB) * 56 + 28 * kh + (p & 3)) * 1024u;
      auto xl = [&](int i) INL { return asfrag(ld128(rws, (unsigned)lane * 16u, (xb ^ ((unsigned)(i & 3) << 10)) + (unsigned)(i & ~3) * 1024u)); };
#pragma unroll
      for (int i = 0; i < PDB; ++i) Xh[i] = xl(i);
      f32x4 acc[4];
#pragma unroll
      for (int c = 0; c < 4; ++c) acc[c] = f32x4{0.f, 0.f, 0.f, 0.f};
      gemm_lds<4, 0, 9, 28, PDB, RATEB>(acc, Xh, lds + MAIN + kh * 4 * KB + lane * 16, xl, [](int s) INL { return s * 8 * KB; }, nohook, rws, dead);
      STAMP(21 + 32 * L);
      BAR();
      load_w2(19, 28, 0, 8);
      gemm_lds<4, 9, 19, 28, PDB, RATEB>(acc, Xh, lds + MAIN + kh * 4 * KB + lane * 16, xl, [](int s) INL { return s * 8 * KB; }, nohook, rws, dead);
      VMWAIT0();
      BAR();
      STAMP(22 + 32 * L);
      auto hookq = [&](int s) INL { if (L == 0 && w < 3 && s == 19 + 2 * w) touch8k(rws, (unsigned)lane * 128u, WS_WQT + (unsigned)(p * 96 + (t & 3) * 24 + w * 8) * 1024u, ldsb + T_DUMP); };
      gemm_lds<4, 19, 28, 28, PDB, RATEB>(acc, Xh, lds + MAIN + kh * 4 * KB + lane * 16, xl, [](int s) INL { return (s - 19) * 8 * KB; }, hookq, rws, dead);
      STAMP(14 + 32 * L);
      FRESH();
      *(LAS f32x4*)(ldsw + L_RED2 + (w * 2 + 0) * KB + lane * 16) = kh ? acc[0] : acc[2];
      *(LAS f32x4*)(ldsw + L_RED2 + (w * 2 + 1) * KB + lane * 16) = kh ? acc[1] : acc[3];
      BAR();
      const f32x4 o0 = *(const LAS f32x4*)(lds + L_RED2 + ((w ^ 4) * 2 + 0) * KB + lane * 16), o1 = *(const LAS f32x4*)(lds + L_RED2 + ((w ^ 4) * 2 + 1) * KB + lane * 16);
      const f32x4 k0 = kh ? acc[2] : acc[0], k1 = kh ? acc[3] : acc[1];
      const f32x4 b0 = *(const LAS f32x4*)(lds + T_B2 + (kh * 32 + 4 * g) * 4), b1v = *(const LAS f32x4*)(lds + T_B2 + (kh * 32 + 16 + 4 * g) * 4);
      float v[8];
      v[0] = k0[0] + o0[0] + b0[0] + bflo(resid.x); v[1] = k0[1] + o0[1] + b0[1] + bfhi(resid.x); v[2] = k0[2] + o0[2] + b0[2] + bflo(resid.y); v[3] = k0[3] + o0[3] + b0[3] + bfhi(resid.y);
      v[4] = k1[0] + o1[0] + b1v[0] + bflo(resid.z); v[5] = k1[1] + o1[1] + b1v[1] + bfhi(resid.z); v[6] = k1[2] + o1[2] + b1v[2] + bflo(resid.w); v[7] = k1[3] + o1[3] + b1v[3] + bfhi(resid.w);
      if (L == 1) {
        const unsigned a0 = cvtpk(v[0], v[1]), a1 = cvtpk(v[2], v[3]), c0 = cvtpk(v[4], v[5]), c1 = cvtpk(v[6], v[7]);
        auto s0 = __builtin_amdgcn_permlane16_swap(a0, c0, false, false);
        auto s1 = __builtin_amdgcn_permlane16_swap(a1, c1, false, false);
        const u32x4 o = {s0[0], s1[0], s0[1], s1[1]};
        const int choff = (g & 1) * 16 + (g >> 1) * 8;
        { const rsrc_t ro = __builtin_amdgcn_make_buffer_rsrc((void*)P.out, 0, S_ * D_ * 2, 0x00020000);
          __builtin_amdgcn_raw_buffer_store_b128(o, ro, (unsigned)(((16 * tauB + tok) * D_ + 32 * ksB + choff) * 2), 0, OUT_AUX); }
        STAMP(15 + 32 * L); RT(60); STAMP_OUT();
        break;
      }
      publish_x(rws, v, WS_X2 + (unsigned)(tauB * 16 + ksB) * 1024u, WS_ST2 + (unsigned)(tauB * 16 + ksB) * 128u, lane, resid);
      VMWAIT0();
      BAR();
      if (w == 0 && lane == 0) set_flag(rws, flag_row(0, K_X2) + (unsigned)(t * 16 + p) * 128u, tag);
      load_wq(1, 7);
      if (w == 0) poll_flags(rws, flag_row(0, K_X2) + (unsigned)(t * 16 + lane) * 128u, 16, tag, lane, dead);
      VMWAIT0();
      BAR();
      STAMP(15 + 32 * L);
    }
  }
}

extern "C" void kernel_launch(void* const* d_in, const int* in_sizes, int n_in, void* d_out, int out_size, void* d_ws, size_t ws_size, hipStream_t stream) {
  Params P;
  P.x = (const u16*)d_in[0]; P.wqkv = (const u16*)d_in[1]; P.wo = (const u16*)d_in[2];
  P.w1 = (const u16*)d_in[4]; P.b1 = (const u16*)d_in[5]; P.w2 = (const u16*)d_in[6]; P.b2 = (const u16*)d_in[7];
  P.out = (u16*)d_out; P.ws = (unsigned char*)d_ws;
  mega<<<256, 512, 0, stream>>>(P);
}
```

```cpp
#include <hip/hip_runtime.h>
#include <stdint.h>

typedef unsigned short u16;
typedef unsigned long long u64;
typedef short bf16x8 __attribute__((ext_vector_type(8)));
typedef short s16x4 __attribute__((ext_vector_type(4)));
typedef float f32x2 __attribute__((ext_vector_type(2)));
typedef float f32x4 __attribute__((ext_vector_type(4)));
typedef float f32x16 __attribute__((ext_vector_type(16)));
typedef unsigned u32x4 __attribute__((ext_vector_type(4)));
typedef unsigned u32x2 __attribute__((ext_vector_type(2)));
typedef __bf16 bf16x2_t __attribute__((ext_vector_type(2)));
#define LAS __attribute__((address_space(3)))
#define GAS __attribute__((address_space(1)))
#define DI __device__ __forceinline__
#define INL __attribute__((always_inline))
typedef __amdgpu_buffer_rsrc_t rsrc_t;

constexpr int S_ = 2048, D_ = 512, F_ = 1792;
constexpr float EPS = 1e-5f;
constexpr float QSCALE = 0.25f * 1.4426950408889634f;

constexpr unsigned WS_EPOCH = 0, WS_ERR = 128, WS_FLAGS = 4096;
enum { K_KV = 0, K_O = 1, K_X1 = 2, K_H = 3, K_X2 = 4 };
constexpr unsigned flag_row(int L, int kind) { return WS_FLAGS + (unsigned)((L * 5 + kind) * 256 * 128); }
constexpr unsigned FL_WT = flag_row(1, K_X2);
constexpr unsigned WS_WQT = 0x60000;
constexpr unsigned WS_WOT = WS_WQT + 16 * 96 * 1024;
constexpr unsigned WS_W1T = WS_WOT + 8 * 64 * 1024;
constexpr unsigned WS_W2T = WS_W1T + 16 * 112 * 1024;
constexpr unsigned WS_KG = WS_W2T + 8 * 224 * 1024;
constexpr unsigned WS_VG = WS_KG + 2 * 256 * 8192;
constexpr unsigned WS_OG = WS_VG + 2 * 256 * 8192;
constexpr unsigned WS_X1 = WS_OG + 2 * 128 * 16 * 1024;
constexpr unsigned WS_ST1 = WS_X1 + 2 * 128 * 16 * 1024;
constexpr unsigned WS_HG = WS_ST1 + 2 * 128 * 16 * 128;
constexpr unsigned WS_X2 = WS_HG + 2 * 128 * 56 * 1024;
constexpr unsigned WS_ST2 = WS_X2 + 128 * 16 * 1024;
constexpr unsigned WS_DBG = WS_ST2 + 128 * 16 * 128;
constexpr unsigned WS_END = WS_DBG + 256 * 8 * 128 * 8;

constexpr int KB = 1024;
constexpr int CODE_BYTES = 36 * 1024;
constexpr int LDS_BYTES = 163840;
constexpr int T_CSQ = 0;
constexpr int T_CS1 = 512;
constexpr int T_B1 = 1024;
constexpr int T_B2 = 1536;
constexpr int T_DUMP = 1792;
constexpr int MAIN = 2048;
constexpr int PLN = 384 * 16 + 64;
constexpr int A_K = MAIN + 96 * KB, A_V = A_K + 4 * PLN, A_Q = MAIN, A_ONE = A_Q + 4 * 2048;
static_assert(A_V + 4 * PLN <= LDS_BYTES && A_ONE + 6400 <= MAIN + 16 * KB, "attention buffers");
constexpr int T_MASK = MAIN + 148 * KB;
static_assert(A_V + 4 * PLN <= T_MASK && T_MASK + 4096 <= LDS_BYTES, "mask table");
constexpr int L_W1 = MAIN + 46 * KB;
constexpr int L_WO = MAIN + 16 * KB;
constexpr int L_PRE = MAIN + 132 * KB;
constexpr int XROW = 1040;
static_assert(128 * XROW <= 132 * KB && L_PRE + 24 * KB <= LDS_BYTES, "prologue scratch");
constexpr int L_RED2 = MAIN + 96 * KB;
static_assert(L_W1 + 112 * KB <= LDS_BYTES && L_WO + 64 * KB <= LDS_BYTES, "images");

template <int V> struct IC { static constexpr int value = V; };
struct Params {
  const u16 *x, *wqkv, *wo, *w1, *b1, *w2, *b2;
  u16* out;
  unsigned char* ws;
};

DI float bf2f(unsigned v) { return __uint_as_float(v << 16); }
DI float bflo(unsigned v) { return __uint_as_float(v << 16); }
DI float bfhi(unsigned v) { return __uint_as_float(v & 0xffff0000u); }
DI unsigned cvtpk(float lo, float hi) { f32x2 v = {lo, hi}; bf16x2_t b = __builtin_convertvector(v, bf16x2_t); return __builtin_bit_cast(unsigned, b); }
DI unsigned cvtpk2(f32x2 v) { bf16x2_t b = __builtin_convertvector(v, bf16x2_t); return __builtin_bit_cast(unsigned, b); }
DI f32x4 mfma16(bf16x8 a, bf16x8 b, f32x4 c) { return __builtin_amdgcn_mfma_f32_16x16x32_bf16(a, b, c, 0, 0, 0); }
DI f32x16 mfma32(bf16x8 a, bf16x8 b, f32x16 c) { return __builtin_amdgcn_mfma_f32_32x32x16_bf16(a, b, c, 0, 0, 0); }
DI bf16x8 ones_frag() { unsigned o = 0x3F803F80u; asm volatile("" : "+v"(o)); return __builtin_bit_cast(bf16x8, u32x4{o, o, o, o}); }
DI bf16x8 asfrag(u32x4 v) { return __builtin_bit_cast(bf16x8, v); }
DI s16x4 trd(const LAS char* p) { return __builtin_bit_cast(s16x4, __builtin_amdgcn_ds_read_tr16_b64_v4i16((LAS s16x4*)p)); }
DI bf16x8 ldsfrag(const LAS char* p) { return *(const LAS bf16x8*)p; }
DI float max3f(float a, float b, float c) { return __builtin_fmaxf(__builtin_fmaxf(a, b), c); }
DI float swap32_add(float x) { auto r = __builtin_amdgcn_permlane32_swap(__float_as_uint(x), __float_as_uint(x), false, false); return __uint_as_float(r[0]) + __uint_as_float(r[1]); }
DI float swap16_add(float x) { auto r = __builtin_amdgcn_permlane16_swap(__float_as_uint(x), __float_as_uint(x), false, false); return __uint_as_float(r[0]) + __uint_as_float(r[1]); }
DI float quad_sum(float x) { return swap32_add(swap16_add(x)); }
DI float swap32_max(float x) { auto r = __builtin_amdgcn_permlane32_swap(__float_as_uint(x), __float_as_uint(x), false, false); return fmaxf(__uint_as_float(r[0]), __uint_as_float(r[1])); }

#define FRESH() do { asm volatile("" : "+v"(lane), "+s"(w), "+s"(t), "+s"(p)); tok = lane & 15; g = lane >> 4; hh = p >> 3; cg = p & 7; tt = w & 3; kh = w >> 2; \
  tauA = 8 * t + w; tauB = 8 * t + 4 * hh + tt; ksB = 2 * cg + kh; u32i = 2 * t + hh; } while (0)
#define PLANE ((g & 1) * 2 + (g >> 1))
#define TL (16 * w + tok)
#define FRESH_V() do { asm volatile("" : "+v"(lane)); tok = lane & 15; g = lane >> 4; } while (0)
#define BAR() asm volatile("s_waitcnt lgkmcnt(0)\n\ts_barrier" ::: "memory")
#define VMWAIT0() asm volatile("s_waitcnt vmcnt(0)" ::: "memory")
#define SBAR() __builtin_amdgcn_sched_barrier(0)
constexpr int SC1 = 16;
#define RATEA 2
#define RATEB 1
#define PDA 4
#define PDB 4
#define OUT_AUX 16
#define XAUX 16
DI u32x4 ld128(rsrc_t r, unsigned voff, unsigned soff) { return __builtin_amdgcn_raw_buffer_load_b128(r, voff, soff, XAUX); }
DI u32x2 ld64(rsrc_t r, unsigned voff, unsigned soff) { return __builtin_amdgcn_raw_buffer_load_b64(r, voff, soff, SC1); }
DI void st128(u32x4 v, rsrc_t r, unsigned voff, unsigned soff) { __builtin_amdgcn_raw_buffer_store_b128(v, r, voff + soff, 0, SC1); }
DI void st64(u32x2 v, rsrc_t r, unsigned voff, unsigned soff) { __builtin_amdgcn_raw_buffer_store_b64(v, r, voff + soff, 0, SC1); }
DI unsigned rfl(unsigned x) { return __builtin_amdgcn_readfirstlane(x); }
DI void dma16(rsrc_t r, unsigned voff, unsigned soff, unsigned ldsaddr) {
  asm volatile("s_mov_b32 m0, %0\n\ts_nop 3\n\tbuffer_load_dwordx4 %1, %2, %3 offen lds" :: "s"(rfl(ldsaddr)), "v"(voff), "s"(r), "s"(rfl(soff)) : "memory");
}
DI void dma4k(rsrc_t r, unsigned voff, unsigned soff, unsigned ldsaddr) {
  asm volatile("s_mov_b32 m0, %0\n\ts_nop 3\n\tbuffer_load_dwordx4 %1, %2, %3 offen sc1 lds\n\tbuffer_load_dwordx4 %1, %2, %3 offen offset:1024 sc1 lds\n\t"
               "buffer_load_dwordx4 %1, %2, %3 offen offset:2048 sc1 lds\n\tbuffer_load_dwordx4 %1, %2, %3 offen offset:3072 sc1 lds" :: "s"(rfl(ldsaddr)), "v"(voff), "s"(r), "s"(rfl(soff)) : "memory");
}
DI void dma3k(rsrc_t r, unsigned voff, unsigned soff, unsigned ldsaddr) {
  asm volatile("s_mov_b32 m0, %0\n\ts_nop 3\n\tbuffer_load_dwordx4 %1, %2, %3 offen sc1 lds\n\tbuffer_load_dwordx4 %1, %2, %3 offen offset:1024 sc1 lds\n\t"
               "buffer_load_dwordx4 %1, %2, %3 offen offset:2048 sc1 lds" :: "s"(rfl(ldsaddr)), "v"(voff), "s"(r), "s"(rfl(soff)) : "memory");
}
DI void dma2k(rsrc_t r, unsigned voff, unsigned soff, unsigned ldsaddr) {
  asm volatile("s_mov_b32 m0, %0\n\ts_nop 3\n\tbuffer_load_dwordx4 %1, %2, %3 offen sc1 lds\n\tbuffer_load_dwordx4 %1, %2, %3 offen offset:1024 sc1 lds" :: "s"(rfl(ldsaddr)), "v"(voff), "s"(r), "s"(rfl(soff)) : "memory");
}
DI void touch8k(rsrc_t r, unsigned voff, unsigned soff, unsigned ldsdump) {
  asm volatile("s_mov_b32 m0, %0\n\ts_nop 3\n\tbuffer_load_dword %1, %2, %3 offen sc1 lds" :: "s"(rfl(ldsdump)), "v"(voff), "s"(r), "s"(rfl(soff)) : "memory");
}

#define POLL_GAP 3
constexpr unsigned SPIN_CAP = 1u << 14;
DI void poll_flags(rsrc_t rws, unsigned voff, int n, u64 tag, int lane, bool& dead) {
  if (dead) return;
  const unsigned vo = lane < n ? voff : WS_EPOCH + 64u;
  const bool idle = lane >= n;
  u32x2 v0, v1, v2;
#define POLL_ISSUE(v) asm volatile("buffer_load_dwordx2 %0, %1, %2, 0 offen sc1" : "=v"(v) : "v"(vo), "s"(rws) : "memory")
#define POLL_OK(v) __all(idle || (v.x == (unsigned)tag && v.y == (unsigned)(tag >> 32)))
  asm volatile("s_waitcnt vmcnt(0)" ::: "memory");
  POLL_ISSUE(v0); __builtin_amdgcn_s_sleep(POLL_GAP);
  POLL_ISSUE(v1); __builtin_amdgcn_s_sleep(POLL_GAP);
  POLL_ISSUE(v2);
  for (unsigned spins = 0;;) {
    asm volatile("s_waitcnt vmcnt(2)" : "+v"(v0) :: "memory");
    if (POLL_OK(v0)) break;
    POLL_ISSUE(v0);
    asm volatile("s_waitcnt vmcnt(2)" : "+v"(v1) :: "memory");
    if (POLL_OK(v1)) break;
    POLL_ISSUE(v1);
    asm volatile("s_waitcnt vmcnt(2)" : "+v"(v2) :: "memory");
    if (POLL_OK(v2)) break;
    POLL_ISSUE(v2);
    if (++spins > SPIN_CAP) { dead = true; break; }
  }
  asm volatile("s_waitcnt vmcnt(0)" : "+v"(v0), "+v"(v1), "+v"(v2) :: "memory");
  if (dead && lane == 0) __builtin_amdgcn_raw_buffer_store_b32(1u, rws, WS_ERR, 0, SC1);
#undef POLL_ISSUE
#undef POLL_OK
}
DI void set_flag(rsrc_t rws, unsigned off, u64 tag) { u32x2 v = {(unsigned)tag, (unsigned)(tag >> 32)}; st64(v, rws, off, 0); }

template <int NP> DI unsigned raw_voff(int j, int lane, unsigned rowpitch, unsigned cb0, unsigned cbs, unsigned gap = 0) {
  const int beta = 2 * j + (lane >> 5), oct = beta / NP, pair = beta % NP, r = (lane >> 2) & 7, q4 = lane & 3;
  return ((unsigned)(8 * oct + r) + (oct >= 2 ? gap : 0u)) * rowpitch + cb0 + (unsigned)pair * cbs + (unsigned)((((q4 >> 1) ^ (r >> 2) ^ oct) & 1) * 32 + (q4 & 1) * 16);
}
template <int NP, int ORD> DI bf16x8 raw_frag(const LAS char* kbase, int ct, int lane) {
  const int g = lane >> 4, q = (lane & 15) >> 2, pp = lane & 3, pair = ct >> 1, c = ct & 1;
  int o1, r1, o2, r2;
  if (ORD == 0) { o1 = g >> 1; r1 = 4 * (g & 1) + q; o2 = o1 + 2; r2 = r1; }
  else { o1 = 2 * (g >> 1); r1 = 4 * (g & 1) + q; o2 = o1 + 1; r2 = r1; }
  const s16x4 lo = trd(kbase + (o1 * NP + pair) * 512 + r1 * 64 + ((c ^ (r1 >> 2) ^ o1) & 1) * 32 + pp * 8);
  const s16x4 hi = trd(kbase + (o2 * NP + pair) * 512 + r2 * 64 + ((c ^ (r2 >> 2) ^ o2) & 1) * 32 + pp * 8);
  return __builtin_shufflevector(lo, hi, 0, 1, 2, 3, 4, 5, 6, 7);
}

template <int NT, int S0, int S1, int NX, int PD, int RATE, class XL, class OFF, class HOOK>
DI void gemm_lds(f32x4 (&acc)[NT], bf16x8 (&X)[NX], const LAS char* wb, XL xload, OFF off, HOOK hook, rsrc_t rws, bool& dead) {
  bf16x8 wf[2][NT];
#pragma unroll
  for (int c = 0; c < NT; ++c) wf[0][c] = ldsfrag(wb + off(S0) + c * 1024);
  SBAR();
#pragma unroll
  for (int s = S0; s < S1; ++s) {
    const int cur = (s - S0) & 1;
    if (PD > 0) {
#pragma unroll
      for (int e = 0; e < RATE; ++e) { const int ix = PD + RATE * s + e; if (ix < NX) X[ix] = xload(ix); }
    }
    hook(s);
    __builtin_amdgcn_s_waitcnt(0xC07F);
    SBAR();
#pragma unroll
    for (int c = 0; c < NT; ++c) {
      if (s + 1 < S1) wf[cur ^ 1][c] = ldsfrag(wb + off(s + 1) + c * 1024);
      acc[c] = mfma16(wf[cur][c], X[s], acc[c]);
    }
#pragma unroll
    for (int c = 0; c < NT; ++c) {
      __builtin_amdgcn_sched_group_barrier(0x008, 1, 0);
      if (s + 1 < S1 && 2 * c < NT) __builtin_amdgcn_sched_group_barrier(0x100, 2, 0);
    }
    SBAR();
  }
}

#define STAMP(i) do {} while (0)
#define RT(i) do {} while (0)
#define STAMP_OUT() do {} while (0)

DI void stats_load(rsrc_t rws, unsigned base, int lane, u32x2 (&pv)[4]) {
  const int tok = lane & 15, g = lane >> 4;
#pragma unroll
  for (int i = 0; i < 4; ++i) pv[i] = ld64(rws, (unsigned)((4 * g + i) * 128 + tok * 8), base);
}
DI void stats_from_partials(const u32x2 (&pv)[4], float& mean, float& var) {
  float s = 0.f;
#pragma unroll
  for (int i = 0; i < 4; ++i) s += __uint_as_float(pv[i].x);
  s = quad_sum(s);
  mean = s * (1.0f / 512.0f);
  float m2 = 0.f;
#pragma unroll
  for (int i = 0; i < 4; ++i) { const float d = __uint_as_float(pv[i].x) * (1.0f / 32.0f) - mean; m2 += __uint_as_float(pv[i].y) + 32.0f * d * d; }
  m2 = quad_sum(m2);
  var = m2 * (1.0f / 512.0f);
}

DI void publish_x(rsrc_t rws, const float (&v)[8], unsigned xoff, unsigned stoff, int lane, u32x4& packed) {
  packed.x = cvtpk(v[0], v[1]); packed.y = cvtpk(v[2], v[3]); packed.z = cvtpk(v[4], v[5]); packed.w = cvtpk(v[6], v[7]);
  st128(packed, rws, (unsigned)lane * 16u, xoff);
  float r[8] = {bflo(packed.x), bfhi(packed.x), bflo(packed.y), bfhi(packed.y), bflo(packed.z), bfhi(packed.z), bflo(packed.w), bfhi(packed.w)};
  float s = 0.f;
#pragma unroll
  for (int i = 0; i < 8; ++i) s += r[i];
  s = quad_sum(s);
  const float mu = s * (1.0f / 32.0f);
  float m2 = 0.f;
#pragma unroll
  for (int i = 0; i < 8; ++i) { const float d = r[i] - mu; m2 += d * d; }
  m2 = quad_sum(m2);
  if (lane < 16) { u32x2 o = {__float_as_uint(s), __float_as_uint(m2)}; st64(o, rws, (unsigned)lane * 8u, stoff); }
}

template <bool FULL, class HOOK>
DI u32x4 attn_wave(const LAS char* lds, int hd, int qi, int jmin, int lane, HOOK hook) {
  const int ql = lane & 31, h = lane >> 5;
  const bf16x8 Q = ldsfrag(lds + A_Q + (hd * 2 + h) * 2048 + (32 * qi + ql) * 16);
  const LAS char* kb = lds + A_K + (hd * 2 + h) * PLN + (32 * qi + ql) * 16;
  const int G = lane >> 4, q4 = (lane & 15) >> 2, pp = lane & 3;
  const LAS char* vb = lds + ((G & 1) ? A_ONE : A_V + (hd * 2 + (pp >> 1)) * PLN) + (32 * qi + 4 * h + q4) * 16 + (pp & 1) * 8;
  constexpr float NEG = -1e30f;
  const f32x16 zero16 = {0.f,0.f,0.f,0.f,0.f,0.f,0.f,0.f,0.f,0.f,0.f,0.f,0.f,0.f,0.f,0.f};
  f32x16 biasD;
  {
    const LAS f32x4* bt = (const LAS f32x4*)(lds + T_MASK + lane * 16);
#pragma unroll
    for (int q = 0; q < 4; ++q) { const f32x4 v = bt[q * 64]; biasD[4 * q] = v[0]; biasD[4 * q + 1] = v[1]; biasD[4 * q + 2] = v[2]; biasD[4 * q + 3] = v[3]; }
  }
  float m0 = NEG, m1 = NEG;
  {
    bf16x8 ka = ldsfrag(kb + 8 * 512), kn = ka;
    if (FULL || 7 >= jmin) kn = ldsfrag(kb + 7 * 512);
    f32x16 sa = mfma32(ka, Q, biasD), sb = sa;
    SBAR();
#pragma unroll
    for (int j = 8; j >= 0; --j) {
      f32x16& cur = (j & 1) ? sb : sa; f32x16& nxt = (j & 1) ? sa : sb;
      if (j >= 1 && (FULL || j - 1 >= jmin)) {
        const bf16x8 kj = kn;
        if (j >= 2 && (FULL || j - 2 >= jmin)) kn = ldsfrag(kb + (j - 2) * 512);
        if (j - 1 == 0) { f32x16 bf;
#pragma unroll
          for (int r = 0; r < 16; ++r) bf[r] = NEG - biasD[r];
          nxt = mfma32(kj, Q, bf); }
        else nxt = mfma32(kj, Q, zero16);
      }
      if (FULL || j >= jmin) {
#pragma unroll
        for (int r = 0; r < 16; r += 4) { m0 = max3f(m0, cur[r], cur[r + 1]); m1 = max3f(m1, cur[r + 2], cur[r + 3]); }
        asm volatile("" : "+v"(m0), "+v"(m1));
      }
      SBAR();
    }
  }
  const float m = swap32_max(fmaxf(m0, m1));
  f32x16 negm;
#pragma unroll
  for (int r = 0; r < 16; ++r) negm[r] = -m;
  f32x16 o = zero16;
  {
    bf16x8 ka = ldsfrag(kb + 8 * 512), kn = ka;
    if (FULL || 7 >= jmin) kn = ldsfrag(kb + 7 * 512);
    f32x16 sa = mfma32(ka, Q, biasD + negm), sb = sa;
    SBAR();
#pragma unroll
    for (int j = 8; j >= 0; --j) {
      f32x16& cur = (j & 1) ? sb : sa; f32x16& nxt = (j & 1) ? sa : sb;
      const bool have = FULL || j >= jmin;
      hook(j);
      s16x4 v0 = {}, v1 = {}, v2 = {}, v3 = {};
      if (have) { v0 = trd(vb + (32 * j) * 16); v1 = trd(vb + (32 * j + 8) * 16); v2 = trd(vb + (32 * j + 16) * 16); v3 = trd(vb + (32 * j + 24) * 16); }
      if (j >= 1 && (FULL || j - 1 >= jmin)) {
        const bf16x8 kj = kn;
        if (j >= 2 && (FULL || j - 2 >= jmin)) kn = ldsfrag(kb + (j - 2) * 512);
        if (j - 1 == 0) { f32x16 cf;
#pragma unroll
          for (int r = 0; r < 16; ++r) cf[r] = (NEG - biasD[r]) - m;
          nxt = mfma32(kj, Q, cf); }
        else nxt = mfma32(kj, Q, negm);
      }
      if (have) {
        unsigned pk[8];
#pragma unroll
        for (int r = 0; r < 8; ++r) pk[r] = cvtpk(__builtin_amdgcn_exp2f(cur[2 * r]), __builtin_amdgcn_exp2f(cur[2 * r + 1]));
        o = mfma32(__builtin_shufflevector(v0, v1, 0, 1, 2, 3, 4, 5, 6, 7), asfrag(u32x4{pk[0], pk[1], pk[2], pk[3]}), o);
        o = mfma32(__builtin_shufflevector(v2, v3, 0, 1, 2, 3, 4, 5, 6, 7), asfrag(u32x4{pk[4], pk[5], pk[6], pk[7]}), o);
      }
      SBAR();
    }
  }
  const float inv = __builtin_amdgcn_rcpf(o[8]);
  return u32x4{cvtpk(o[0] * inv, o[1] * inv), cvtpk(o[2] * inv, o[3] * inv), cvtpk(o[4] * inv, o[5] * inv), cvtpk(o[6] * inv, o[7] * inv)};
}

__global__ __launch_bounds__(512, 2) void mega(Params P) {
  __shared__ __attribute__((aligned(16))) char lds_[LDS_BYTES];
  const LAS char* lds = (const LAS char*)lds_;
  LAS char* ldsw = (LAS char*)lds_;
  const unsigned ldsb = (unsigned)(size_t)lds_;
  int w = rfl(threadIdx.x >> 6);
  int lane = (int)__builtin_amdgcn_mbcnt_hi(~0u, __builtin_amdgcn_mbcnt_lo(~0u, 0u));
  const int b = blockIdx.x;
  int t = 4 * ((b & 7) >> 1) + (b >> 6), p = 8 * (b & 1) + ((b >> 3) & 7);
  int hh = p >> 3, cg = p & 7;
  int tt = w & 3, kh = w >> 2;
  int tauA = 8 * t + w, tauB = 8 * t + 4 * hh + tt, ksB = 2 * cg + kh, u32i = 2 * t + hh;
  int tok = lane & 15, g = lane >> 4;
  const rsrc_t rws = __builtin_amdgcn_make_buffer_rsrc(P.ws, 0, (int)WS_END, 0x00020000);
  bool dead = false;
  STAMP(0); RT(59);
  if (w == 7) {
    u64 pc;
    asm volatile("s_getpc_b64 %0" : "=s"(pc));
    const rsrc_t rc = __builtin_amdgcn_make_buffer_rsrc((void*)pc, 0, CODE_BYTES, 0x00020000);
    touch8k(rc, (unsigned)lane * 128u, (unsigned)((b >> 3) % 5) * 8192u, ldsb + T_DUMP);
    touch8k(rws, (unsigned)lane << 20, 0u, ldsb + T_DUMP);
  }
  u64 tag = 0;
  u32x4 resid = {0u, 0u, 0u, 0u};
  auto nohook = [](int) INL {};
  auto noload = [](int) INL { return bf16x8{}; };

  int nL = 2;
  asm volatile("" : "+s"(nL));
#pragma unroll 1
  for (int L = 0; L < nL; ++L) {
    asm volatile("" : "+s"(L));
    auto load_wq = [&](int w0, int nw) INL {
      if (w >= w0) for (int sl = w - w0; sl < 16; sl += nw) { const unsigned src = WS_WQT + (unsigned)((p * 16 + (sl ^ (2 * (p & 7)))) * 6) * 1024u, dst = ldsb + MAIN + (unsigned)sl * 6144u;
        dma4k(rws, (unsigned)lane * 16u, src, dst); dma2k(rws, (unsigned)lane * 16u, src + 4096u, dst + 4096u); }
    };
    auto load_w1 = [&](int s0, int s1, int w0, int nw) INL {
      if (w >= w0) for (int sl = s0 + w - w0; sl < s1; sl += nw) { const unsigned src = WS_W1T + (unsigned)((p * 16 + (sl ^ (2 * (p & 7)))) * 7) * 1024u, dst = ldsb + L_W1 + (unsigned)sl * 7168u;
        dma4k(rws, (unsigned)lane * 16u, src, dst); dma3k(rws, (unsigned)lane * 16u, src + 4096u, dst + 4096u); }
    };
    auto w2_half = [&](int i, int e) INL {
      const int slot = i >= 19 ? i - 19 : i, k = i ^ (p & 3);
      dma4k(rws, (unsigned)lane * 16u, WS_W2T + (unsigned)((cg * 56 + e * 28 + k) * 4) * 1024u, ldsb + MAIN + (unsigned)(slot * 8 + e * 4) * 1024u);
    };
    auto load_w2 = [&](int i0, int i1, int w0, int nw) INL {
      if (w >= w0) for (int q = 2 * i0 + w - w0; q < 2 * i1; q += nw) w2_half(q >> 1, q & 1);
    };
    FRESH();
    {
      bf16x8 X[16];
      float mean = 0.f, var = 0.f;
      const int rotA = 2 * (p & 7);
      f32x4 akv[4];
#pragma unroll
      for (int c = 0; c < 4; ++c) akv[c] = f32x4{0.f, 0.f, 0.f, 0.f};
      if (L == 0) {
        const rsrc_t rx = __builtin_amdgcn_make_buffer_rsrc((void*)P.x, 0, S_ * D_ * 2, 0x00020000);
        if (w < 2) {
          const rsrc_t rq = __builtin_amdgcn_make_buffer_rsrc((void*)P.wqkv, 0, D_ * 3 * D_ * 2, 0x00020000);
#pragma unroll
          for (int jj = 0; jj < 3; ++jj) { const int j = 3 * w + jj; dma16(rq, raw_voff<3>(j, lane, 3072u, (unsigned)p * 64u, 1024u), rfl((unsigned)t * 32u * 3072u), rfl(ldsb + L_PRE + 18 * KB + (unsigned)j * 1024u)); }
        } else if (w < 4) {
          {
            const rsrc_t r1 = __builtin_amdgcn_make_buffer_rsrc((void*)P.w1, 0, D_ * F_ * 2, 0x00020000);
#pragma unroll
            for (int jj = 0; jj < 4; ++jj) { const int j = 4 * (w - 2) + jj; dma16(r1, raw_voff<4>(j, lane, 3584u, (unsigned)p * 224u, 64u), rfl((unsigned)t * 32u * 3584u), rfl(ldsb + L_PRE + (unsigned)j * 1024u)); }
          }
        } else if (w == 4) {
          const rsrc_t ro = __builtin_amdgcn_make_buffer_rsrc((void*)P.wo, 0, D_ * D_ * 2, 0x00020000);
#pragma unroll
          for (int j = 0; j < 2; ++j) dma16(ro, raw_voff<1>(j, lane, 1024u, (unsigned)(cg * 128 + (u32i & 1) * 64), 0u), rfl((unsigned)(u32i >> 1) * 32u * 1024u), rfl(ldsb + L_PRE + 8 * KB + (unsigned)j * 1024u));
        } else if (w == 5 || (w == 6 && u32i < 24)) {
          const rsrc_t r2 = __builtin_amdgcn_make_buffer_rsrc((void*)P.w2, 0, F_ * D_ * 2, 0x00020000);
          const int ks = u32i + (w - 5) * 32, k2h = ks >= 28 ? 1 : 0, ki = ks - 28 * k2h;
          const int row0 = ki < 24 ? 112 * (8 * k2h + ki / 3) + 32 * (ki % 3) : 112 * (8 * k2h + 2 * (ki - 24)) + 96;
#pragma unroll
          for (int j = 0; j < 4; ++j) dma16(r2, raw_voff<2>(j, lane, 1024u, (unsigned)cg * 128u, 64u, ki < 24 ? 0u : 96u), rfl((unsigned)row0 * 1024u), rfl(ldsb + L_PRE + (10 + (w - 5) * 4) * KB + (unsigned)j * 1024u));
        }
#pragma unroll
        for (int r = 0; r < 16; ++r) { const int rr = r ^ (2 * (p & 7)); dma16(rx, (unsigned)lane * 16u, (unsigned)(16 * tauA + rr) * 1024u, ldsb + MAIN + (unsigned)((16 * w + rr) * XROW)); }
        u32x2 epoch_raw = ld64(rws, WS_EPOCH, 0);
        STAMP(23);
        VMWAIT0();
        tag = ((((u64)rfl(epoch_raw.y)) << 32) | rfl(epoch_raw.x)) + 1ull;
        BAR();
        STAMP(1);
        if (w < 6) st128(__builtin_bit_cast(u32x4, raw_frag<3, 0>(lds + L_PRE + 18 * KB, w, lane)), rws, (unsigned)lane * 16u, rfl(WS_WQT + (unsigned)((p * 16 + t) * 6 + w) * 1024u));
        VMWAIT0();
        BAR();
        if (w == 0 && lane == 0) set_flag(rws, FL_WT + (unsigned)(t * 16 + p) * 128u, tag);
        STAMP(2);
        FRESH_V();
        if (w < 7) st128(__builtin_bit_cast(u32x4, raw_frag<4, 0>(lds + L_PRE, w, lane)), rws, (unsigned)lane * 16u, rfl(WS_W1T + (unsigned)((p * 16 + t) * 7 + w) * 1024u));
        if (w >= 6) st128(__builtin_bit_cast(u32x4, raw_frag<1, 1>(lds + L_PRE + 8 * KB, w - 6, lane)), rws, (unsigned)lane * 16u, rfl(WS_WOT + (unsigned)((cg * 16 + (u32i >> 1)) * 4 + (u32i & 1) * 2 + (w - 6)) * 1024u));
        if (w < 4 || u32i < 24) { const int hs = w >> 2, ks = u32i + hs * 32;
          st128(__builtin_bit_cast(u32x4, raw_frag<2, 0>(lds + L_PRE + (10 + hs * 4) * KB, w & 3, lane)), rws, (unsigned)lane * 16u, rfl(WS_W2T + (unsigned)((cg * 56 + ks) * 4 + (w & 3)) * 1024u)); }
        {
          const LAS char* xp = lds + MAIN + (16 * w + tok) * XROW + g * 8;
#pragma unroll
          for (int s = 0; s < 16; ++s) { const int ks = s ^ rotA; const u32x2 lo = *(const LAS u32x2*)(xp + ks * 64), hi = *(const LAS u32x2*)(xp + ks * 64 + 32); X[s] = asfrag(u32x4{lo.x, lo.y, hi.x, hi.y}); }
          f32x4 s1 = {0.f, 0.f, 0.f, 0.f}, s2 = {0.f, 0.f, 0.f, 0.f};
          const bf16x8 on = ones_frag();
#pragma unroll
          for (int i = 0; i < 16; ++i) { s1 = mfma16(on, X[i], s1); s2 = mfma16(X[i], X[i], s2); }
          mean = s1[0] * (1.0f / 512.0f);
          const int dj = tok - 4 * g;
          const float dv = dj == 0 ? s2[0] : dj == 1 ? s2[1] : dj == 2 ? s2[2] : dj == 3 ? s2[3] : 0.f;
          var = fmaxf(quad_sum(dv) * (1.0f / 512.0f) - mean * mean, 0.f);
        }
        if (w == 0) poll_flags(rws, FL_WT + (unsigned)((lane * 16 + p) * 128), 16, tag, lane, dead);
        BAR();
        STAMP(24);
        load_wq(0, 8);
        unsigned biasv = 0;
        if (w < 2) { if (w * 64 + lane < 112) biasv = P.b1[p * 112 + w * 64 + lane]; } else if (w == 2) biasv = P.b2[cg * 64 + lane];
        {
          const GAS u32x2* rp = (const GAS u32x2*)(P.x + (size_t)(16 * tauB + tok) * D_ + 32 * ksB + 4 * g);
          const u32x2 r0 = rp[0], r1 = rp[4];
          resid = u32x4{r0.x, r0.y, r1.x, r1.y};
        }
        if (w < 3) *(LAS float*)(ldsw + (w < 2 ? T_B1 + (w * 64 + lane) * 4 : T_B2 + lane * 4)) = bf2f(biasv);
        VMWAIT0();
        BAR();
        STAMP(27);
        if (w < 6) {
          f32x4 cs = {0.f, 0.f, 0.f, 0.f};
#pragma unroll
          for (int s = 0; s < 16; ++s) cs = mfma16(ldsfrag(lds + MAIN + (s * 6 + w) * KB + lane * 16), ones_frag(), cs);
          if (tok == 0) *(LAS f32x4*)(ldsw + T_CSQ + (w * 16 + 4 * g) * 4) = cs;
        }
        STAMP(16 + 32 * L);
        gemm_lds<4, 0, 16, 16, 0, 2>(akv, X, lds + MAIN + 2 * KB + lane * 16, noload, [](int s) INL { return s * 6 * KB; }, nohook, rws, dead);
        BAR();
      } else {
        STAMP(16 + 32 * L);
        u32x2 pv[4];
        const unsigned xb = WS_X2 + (unsigned)(tauA * 16 + rotA) * 1024u;
        auto xl = [&](int s) INL { return asfrag(ld128(rws, (unsigned)lane * 16u, xb ^ ((unsigned)s << 10))); };
#pragma unroll
        for (int s = 0; s < PDA; ++s) X[s] = xl(s);
        auto hs = [&](int s) INL { if (s == 10) stats_load(rws, WS_ST2 + (unsigned)tauA * 2048u, lane, pv); };
        gemm_lds<4, 0, 16, 16, PDA, RATEA>(akv, X, lds + MAIN + 2 * KB + lane * 16, xl, [](int s) INL { return s * 6 * KB; }, hs, rws, dead);
        stats_from_partials(pv, mean, var);
      }
      FRESH();
      const float rs = __builtin_amdgcn_rsqf(var + EPS), rm = rs * mean;
      auto qkv_pack = [&](const f32x4& a0, const f32x4& a1, int c0, float sc) INL {
        const f32x4 cs0 = *(const LAS f32x4*)(lds + T_CSQ + (c0 * 16 + 4 * g) * 4), cs1 = *(const LAS f32x4*)(lds + T_CSQ + (c0 * 16 + 16 + 4 * g) * 4);
        float v[8];
#pragma unroll
        for (int j = 0; j < 4; ++j) { v[j] = (rs * a0[j] - rm * cs0[j]) * sc; v[4 + j] = (rs * a1[j] - rm * cs1[j]) * sc; }
        auto s0 = __builtin_amdgcn_permlane16_swap(cvtpk(v[0], v[1]), cvtpk(v[4], v[5]), false, false);
        auto s1 = __builtin_amdgcn_permlane16_swap(cvtpk(v[2], v[3]), cvtpk(v[6], v[7]), false, false);
        return u32x4{s0[0], s1[0], s0[1], s1[1]};
      };
      {
        const u32x4 ko = qkv_pack(akv[0], akv[1], 2, 1.0f), vo = qkv_pack(akv[2], akv[3], 4, 1.0f);
        st128(ko, rws, (unsigned)(PLANE * 2048 + TL * 16), WS_KG + (unsigned)((L * 16 + t) * 16 + p) * 8192u);
        st128(vo, rws, (unsigned)(PLANE * 2048 + TL * 16), WS_VG + (unsigned)((L * 16 + t) * 16 + p) * 8192u);
        *(LAS u32x4*)(ldsw + A_K + PLANE * PLN + (256 + TL) * 16) = ko;
        *(LAS u32x4*)(ldsw + A_V + PLANE * PLN + (256 + TL) * 16) = vo;
      }
      STAMP(3 + 32 * L);
      FRESH();
      f32x4 aq[2] = {f32x4{0.f, 0.f, 0.f, 0.f}, f32x4{0.f, 0.f, 0.f, 0.f}};
      gemm_lds<2, 0, 8, 16, 0, 2>(aq, X, lds + MAIN + lane * 16, noload, [](int s) INL { return s * 6 * KB; }, nohook, rws, dead);
      VMWAIT0();
      BAR();
      if (w == 0 && lane == 0) set_flag(rws, flag_row(0, K_KV) + (unsigned)(L * 5 * 256 + t * 16 + p) * 128u, tag);
      gemm_lds<2, 8, 16, 16, 0, 2>(aq, X, lds + MAIN + lane * 16, noload, [](int s) INL { return s * 6 * KB; }, nohook, rws, dead);
      STAMP(4 + 32 * L);
      FRESH();
      const u32x4 qo = qkv_pack(aq[0], aq[1], 0, QSCALE);
      if (w == 0) {
        if (L == 0) {
          const int grp = lane >> 4, pg = grp == 0 ? p : cg + 8 * (grp - 1);
          poll_flags(rws, flag_row(0, K_KV) + (unsigned)(((lane & 15) * 16 + pg) * 128), 48, tag, lane, dead);
        } else poll_flags(rws, flag_row(1, K_KV) + (unsigned)(((t - 1 - lane) * 16 + p) * 128), t < 2 ? t : 2, tag, lane, dead);
      }
      BAR();
      STAMP(5 + 32 * L);
      FRESH();
      {
        const int d = 1 + (w >> 2);
        u32x4 hv[4] = {u32x4{}, u32x4{}, u32x4{}, u32x4{}};
        if (t - d >= 0) {
          const unsigned hb = ((w & 2) ? WS_VG : WS_KG) + (unsigned)((L * 16 + t - d) * 16 + p) * 8192u + (unsigned)(w & 1) * 4096u;
#pragma unroll
          for (int i = 0; i < 4; ++i) hv[i] = ld128(rws, (unsigned)lane * 16u, hb + (unsigned)i * 1024u);
        }
        *(LAS u32x4*)(ldsw + A_Q + PLANE * 2048 + TL * 16) = qo;
        if (w * 64 + lane < 400) *(LAS u32x4*)(ldsw + A_ONE + (w * 64 + lane) * 16) = __builtin_bit_cast(u32x4, ones_frag());
        { const int r0 = 4 * (w & 3) + 2 * (w >> 2), key = (r0 & 3) + 8 * (r0 >> 2), dq = (lane & 31) - 4 * (lane >> 5);
          *(LAS f32x2*)(ldsw + T_MASK + (w & 3) * 1024 + lane * 16 + (w >> 2) * 8) = f32x2{key <= dq ? 0.f : -1e30f, key + 1 <= dq ? 0.f : -1e30f}; }
        if (t - d >= 0) {
#pragma unroll
          for (int i = 0; i < 4; ++i) *(LAS u32x4*)(ldsw + ((w & 2) ? A_V : A_K) + (2 * (w & 1) + (i >> 1)) * PLN + ((2 - d) * 128 + (i & 1) * 64 + lane) * 16) = hv[i];
        }
      }
      BAR();
      STAMP(6 + 32 * L);
    }
    FRESH();
    {
      auto hook = [&](int j) INL {
        if (w < 4 && j == 8 - 2 * w) touch8k(rws, (unsigned)lane * 128u, WS_W1T + (unsigned)(p * 112 + (t & 3) * 28 + w * 8) * 1024u, ldsb + T_DUMP);
        if (w < 2 && j == 7 - 6 * w) touch8k(rws, (unsigned)lane * 128u, WS_WOT + (unsigned)(cg * 64 + (t & 3) * 16 + w * 8) * 1024u, ldsb + T_DUMP);
      };
      STAMP(17 + 32 * L);
      u32x4 ov;
      if (t >= 2) ov = attn_wave<true>(lds, w & 1, w >> 1, 0, lane, hook); else ov = attn_wave<false>(lds, w & 1, w >> 1, 8 - 4 * t - (w >> 1), lane, hook);
      FRESH();
      st128(ov, rws, (unsigned)(((lane & 31) >> 4) * 16384 + ((2 * (w & 1) + (lane >> 5)) * 16 + (lane & 15)) * 16), WS_OG + (unsigned)((L * 128 + 8 * t + 2 * (w >> 1)) * 16 + p) * 1024u);
      STAMP(18 + 32 * L);
      if (L == 1 && b == 255 && w == 1) {
        bool d2 = dead;
#pragma unroll 1
        for (int i = 0; i < 4; ++i) poll_flags(rws, FL_WT + (unsigned)(i * 64 + lane) * 128u, 64, tag, lane, d2);
        if (lane == 0) set_flag(rws, WS_EPOCH, tag);
      }
      if (w < 4) {
#pragma unroll
        for (int i = 0; i < 4; ++i) { const int sl = w + 4 * i; dma4k(rws, (unsigned)lane * 16u, WS_WOT + (unsigned)((cg * 16 + (sl ^ cg)) * 4) * 1024u, ldsb + L_WO + (unsigned)sl * 4096u); }
      }
      VMWAIT0();
      BAR();
      if (w == 0 && lane == 0) set_flag(rws, flag_row(0, K_O) + (unsigned)(L * 5 * 256 + t * 16 + p) * 128u, tag);
      STAMP(7 + 32 * L);
      load_w1(5, 16, 1, 7);
      if (w == 0) poll_flags(rws, flag_row(0, K_O) + (unsigned)(L * 5 * 256 + t * 16 + lane) * 128u, 16, tag, lane, dead);
      BAR();
      STAMP(8 + 32 * L);
    }
    FRESH();
    {
      bf16x8 Xo[8];
      const unsigned xb = WS_OG + (unsigned)((L * 128 + tauB) * 16 + 8 * kh + cg) * 1024u;
      auto xl = [&](int i) INL { return asfrag(ld128(rws, (unsigned)lane * 16u, xb ^ ((unsigned)i << 10))); };
#pragma unroll
      for (int i = 0; i < 8; ++i) Xo[i] = xl(i);
      f32x4 acc[4];
#pragma unroll
      for (int c = 0; c < 4; ++c) acc[c] = f32x4{0.f, 0.f, 0.f, 0.f};
      gemm_lds<4, 0, 8, 8, 0, 2>(acc, Xo, lds + L_WO + kh * 32 * KB + lane * 16, xl, [](int s) INL { return s * 4 * KB; }, nohook, rws, dead);
      STAMP(19 + 32 * L);
      FRESH();
      *(LAS f32x4*)(ldsw + MAIN + (w * 2 + 0) * KB + lane * 16) = kh ? acc[0] : acc[2];
      *(LAS f32x4*)(ldsw + MAIN + (w * 2 + 1) * KB + lane * 16) = kh ? acc[1] : acc[3];
      BAR();
      const f32x4 o0 = *(const LAS f32x4*)(lds + MAIN + ((w ^ 4) * 2 + 0) * KB + lane * 16), o1 = *(const LAS f32x4*)(lds + MAIN + ((w ^ 4) * 2 + 1) * KB + lane * 16);
      const f32x4 k0 = kh ? acc[2] : acc[0], k1 = kh ? acc[3] : acc[1];
      float v[8];
      v[0] = k0[0] + o0[0] + bflo(resid.x); v[1] = k0[1] + o0[1] + bfhi(resid.x); v[2] = k0[2] + o0[2] + bflo(resid.y); v[3] = k0[3] + o0[3] + bfhi(resid.y);
      v[4] = k1[0] + o1[0] + bflo(resid.z); v[5] = k1[1] + o1[1] + bfhi(resid.z); v[6] = k1[2] + o1[2] + bflo(resid.w); v[7] = k1[3] + o1[3] + bfhi(resid.w);
      publish_x(rws, v, WS_X1 + (unsigned)((L * 128 + tauB) * 16 + ksB) * 1024u, WS_ST1 + (unsigned)((L * 128 + tauB) * 16 + ksB) * 128u, lane, resid);
      VMWAIT0();
      BAR();
      if (w == 0 && lane == 0) set_flag(rws, flag_row(0, K_X1) + (unsigned)(L * 5 * 256 + t * 16 + p) * 128u, tag);
      STAMP(9 + 32 * L);
      load_w1(0, 5, 1, 7);
      if (w == 0) poll_flags(rws, flag_row(0, K_X1) + (unsigned)(L * 5 * 256 + t * 16 + lane) * 128u, 16, tag, lane, dead);
      VMWAIT0();
      BAR();
      STAMP(10 + 32 * L);
    }
    FRESH();
    {
      bf16x8 X1[16];
      u32x2 pv1[4];
      const unsigned xb = WS_X1 + (unsigned)((L * 128 + tauA) * 16 + 2 * (p & 7)) * 1024u;
      auto xl = [&](int s) INL { return asfrag(ld128(rws, (unsigned)lane * 16u, xb ^ ((unsigned)s << 10))); };
#pragma unroll
      for (int s = 0; s < PDA; ++s) X1[s] = xl(s);
      if (L == 0 && w < 7) {
        f32x4 cs = {0.f, 0.f, 0.f, 0.f};
#pragma unroll
        for (int s = 0; s < 16; ++s) cs = mfma16(ldsfrag(lds + L_W1 + (s * 7 + w) * KB + lane * 16), ones_frag(), cs);
        if (tok == 0) *(LAS f32x4*)(ldsw + T_CS1 + (w * 16 + 4 * g) * 4) = cs;
      }
      STAMP(26 + 32 * L);
      f32x4 acc[7];
#pragma unroll
      for (int c = 0; c < 7; ++c) acc[c] = f32x4{0.f, 0.f, 0.f, 0.f};
      auto hook = [&](int s) INL {
        if (s == 13) stats_load(rws, WS_ST1 + (unsigned)(L * 128 + tauA) * 2048u, lane, pv1);
      };
      gemm_lds<7, 0, 16, 16, PDA, RATEA>(acc, X1, lds + L_W1 + lane * 16, xl, [](int s) INL { return s * 7 * KB; }, hook, rws, dead);
      float mean1, var1;
      stats_from_partials(pv1, mean1, var1);
      w2_half(w >> 1, w & 1);
      if (w < 2) w2_half(4, w);
      BAR();
      STAMP(11 + 32 * L);
      FRESH_V();
      const float r1 = __builtin_amdgcn_rsqf(var1 + EPS), r2 = __builtin_amdgcn_rsqf(var1 * r1 * r1 + EPS), rs = r1 * r2, rm = rs * mean1;
      constexpr float C1 = -2.0f * 0.7978845608028654f * 1.4426950408889634f, C2 = C1 * 0.044715f;
      const f32x2 rs2 = {rs, rs}, nrm2 = {-rm, -rm}, c1v = {C1, C1}, c2v = {C2, C2}, one2 = {1.0f, 1.0f};
      auto gelu4 = [&](const f32x4& ac, int c, unsigned& o0, unsigned& o1) INL {
        const f32x4 cs = *(const LAS f32x4*)(lds + T_CS1 + (c * 16 + 4 * g) * 4), bb = *(const LAS f32x4*)(lds + T_B1 + (c * 16 + 4 * g) * 4);
        const f32x2 a0 = rs2 * f32x2{ac[0], ac[1]} + (nrm2 * f32x2{cs[0], cs[1]} + f32x2{bb[0], bb[1]}), a1 = rs2 * f32x2{ac[2], ac[3]} + (nrm2 * f32x2{cs[2], cs[3]} + f32x2{bb[2], bb[3]});
        f32x2 e0 = a0 * (c2v * (a0 * a0) + c1v), e1 = a1 * (c2v * (a1 * a1) + c1v);
        e0 = f32x2{__builtin_amdgcn_exp2f(e0.x), __builtin_amdgcn_exp2f(e0.y)} + one2; e1 = f32x2{__builtin_amdgcn_exp2f(e1.x), __builtin_amdgcn_exp2f(e1.y)} + one2;
        e0 = a0 * f32x2{__builtin_amdgcn_rcpf(e0.x), __builtin_amdgcn_rcpf(e0.y)}; e1 = a1 * f32x2{__builtin_amdgcn_rcpf(e1.x), __builtin_amdgcn_rcpf(e1.y)};
        o0 = cvtpk2(e0); o1 = cvtpk2(e1);
      };
      const unsigned hb = WS_HG + (unsigned)((L * 128 + tauA) * 56 + 28 * (p >> 3)) * 1024u;
#pragma unroll
      for (int u = 0; u < 3; ++u) {
        u32x4 o; unsigned x0, x1, x2, x3;
        gelu4(acc[2 * u], 2 * u, x0, x1); gelu4(acc[2 * u + 1], 2 * u + 1, x2, x3);
        o = u32x4{x0, x1, x2, x3};
        st128(o, rws, (unsigned)lane * 16u, hb + (unsigned)(3 * (p & 7) + u) * 1024u);
        SBAR();
        w2_half(5 + 4 * u + (w >> 1), w & 1);
        SBAR();
      }
      {
        unsigned x0, x1;
        gelu4(acc[6], 6, x0, x1);
        st64(u32x2{x0, x1}, rws, (unsigned)lane * 8u, hb + (unsigned)(24 + ((p & 7) >> 1)) * 1024u + (unsigned)(p & 1) * 512u);
        SBAR();
        if (w < 4) w2_half(17 + (w >> 1), w & 1);
      }
      STAMP(20 + 32 * L);
      if (w < 4) asm volatile("s_waitcnt vmcnt(4)" ::: "memory"); else VMWAIT0();
      BAR();
      if (w == 0 && lane == 0) set_flag(rws, flag_row(0, K_H) + (unsigned)(L * 5 * 256 + t * 16 + p) * 128u, tag);
    }
    FRESH();
    {
      STAMP(12 + 32 * L);
      if (w == 0) poll_flags(rws, flag_row(0, K_H) + (unsigned)(L * 5 * 256 + t * 16 + lane) * 128u, 16, tag, lane, dead);
      VMWAIT0();
      BAR();
      STAMP(13 + 32 * L);
      bf16x8 Xh[28];
      const unsigned xb = WS_HG + (unsigned)((L * 128 + tauB) * 56 + 28 * kh + (p & 3)) * 1024u;
      auto xl = [&](int i) INL {
        const unsigned o = (xb ^ ((unsigned)(i & 3) << 10)) + (unsigned)(i & ~3) * 1024u;
        if (i < 24) return asfrag(ld128(rws, (unsigned)lane * 16u, o));
        const u32x2 lo = ld64(rws, (unsigned)lane * 8u, o), hi = ld64(rws, (unsigned)lane * 8u, o + 512u);
        return asfrag(u32x4{lo.x, lo.y, hi.x, hi.y});
      };
#pragma unroll
      for (int i = 0; i < PDB; ++i) Xh[i] = xl(i);
      f32x4 acc[4];
#pragma unroll
      for (int c = 0; c < 4; ++c) acc[c] = f32x4{0.f, 0.f, 0.f, 0.f};
      gemm_lds<4, 0, 9, 28, PDB, RATEB>(acc, Xh, lds + MAIN + kh * 4 * KB + lane * 16, xl, [](int s) INL { return s * 8 * KB; }, nohook, rws, dead);
      STAMP(21 + 32 * L);
      BAR();
      load_w2(19, 28, 0, 8);
      gemm_lds<4, 9, 19, 28, PDB, RATEB>(acc, Xh, lds + MAIN + kh * 4 * KB + lane * 16, xl, [](int s) INL { return s * 8 * KB; }, nohook, rws, dead);
      VMWAIT0();
      BAR();
      STAMP(22 + 32 * L);
      auto hookq = [&](int s) INL { if (L == 0 && w < 3 && s == 19 + 2 * w) touch8k(rws, (unsigned)lane * 128u, WS_WQT + (unsigned)(p * 96 + (t & 3) * 24 + w * 8) * 1024u, ldsb + T_DUMP); };
      gemm_lds<4, 19, 28, 28, PDB, RATEB>(acc, Xh, lds + MAIN + kh * 4 * KB + lane * 16, xl, [](int s) INL { return (s - 19) * 8 * KB; }, hookq, rws, dead);
      STAMP(14 + 32 * L);
      FRESH();
      *(LAS f32x4*)(ldsw + L_RED2 + (w * 2 + 0) * KB + lane * 16) = kh ? acc[0] : acc[2];
      *(LAS f32x4*)(ldsw + L_RED2 + (w * 2 + 1) * KB + lane * 16) = kh ? acc[1] : acc[3];
      BAR();
      const f32x4 o0 = *(const LAS f32x4*)(lds + L_RED2 + ((w ^ 4) * 2 + 0) * KB + lane * 16), o1 = *(const LAS f32x4*)(lds + L_RED2 + ((w ^ 4) * 2 + 1) * KB + lane * 16);
      const f32x4 k0 = kh ? acc[2] : acc[0], k1 = kh ? acc[3] : acc[1];
      const f32x4 b0 = *(const LAS f32x4*)(lds + T_B2 + (kh * 32 + 4 * g) * 4), b1v = *(const LAS f32x4*)(lds + T_B2 + (kh * 32 + 16 + 4 * g) * 4);
      float v[8];
      v[0] = k0[0] + o0[0] + b0[0] + bflo(resid.x); v[1] = k0[1] + o0[1] + b0[1] + bfhi(resid.x); v[2] = k0[2] + o0[2] + b0[2] + bflo(resid.y); v[3] = k0[3] + o0[3] + b0[3] + bfhi(resid.y);
      v[4] = k1[0] + o1[0] + b1v[0] + bflo(resid.z); v[5] = k1[1] + o1[1] + b1v[1] + bfhi(resid.z); v[6] = k1[2] + o1[2] + b1v[2] + bflo(resid.w); v[7] = k1[3] + o1[3] + b1v[3] + bfhi(resid.w);
      if (L == 1) {
        const unsigned a0 = cvtpk(v[0], v[1]), a1 = cvtpk(v[2], v[3]), c0 = cvtpk(v[4], v[5]), c1 = cvtpk(v[6], v[7]);
        auto s0 = __builtin_amdgcn_permlane16_swap(a0, c0, false, false);
        auto s1 = __builtin_amdgcn_permlane16_swap(a1, c1, false, false);
        const u32x4 o = {s0[0], s1[0], s0[1], s1[1]};
        const int choff = (g & 1) * 16 + (g >> 1) * 8;
        { const rsrc_t ro = __builtin_amdgcn_make_buffer_rsrc((void*)P.out, 0, S_ * D_ * 2, 0x00020000);
          __builtin_amdgcn_raw_buffer_store_b128(o, ro, (unsigned)(((16 * tauB + tok) * D_ + 32 * ksB + choff) * 2), 0, OUT_AUX); }
        STAMP(15 + 32 * L); RT(60); STAMP_OUT();
        break;
      }
      publish_x(rws, v, WS_X2 + (unsigned)(tauB * 16 + ksB) * 1024u, WS_ST2 + (unsigned)(tauB * 16 + ksB) * 128u, lane, resid);
      VMWAIT0();
      BAR();
      if (w == 0 && lane == 0) set_flag(rws, flag_row(0, K_X2) + (unsigned)(t * 16 + p) * 128u, tag);
      load_wq(1, 7);
      if (w == 0) poll_flags(rws, flag_row(0, K_X2) + (unsigned)(t * 16 + lane) * 128u, 16, tag, lane, dead);
      VMWAIT0();
      BAR();
      STAMP(15 + 32 * L);
    }
  }
}

extern "C" void kernel_launch(void* const* d_in, const int* in_sizes, int n_in, void* d_out, int out_size, void* d_ws, size_t ws_size, hipStream_t stream) {
  Params P;
  P.x = (const u16*)d_in[0]; P.wqkv = (const u16*)d_in[1]; P.wo = (const u16*)d_in[2];
  P.w1 = (const u16*)d_in[4]; P.b1 = (const u16*)d_in[5]; P.w2 = (const u16*)d_in[6]; P.b2 = (const u16*)d_in[7];
  P.out = (u16*)d_out; P.ws = (unsigned char*)d_ws;
  mega<<<256, 512, 0, stream>>>(P);
}
```
